```python
import torch

def _softmax(x, axis=-1):
    return torch.softmax(x, dim=axis)
def _gelu(x):
    return torch.nn.functional.gelu(x, approximate="tanh")
def _silu(x):
    return torch.nn.functional.silu(x)
def _sigmoid(x):
    return torch.sigmoid(x)
def _layernorm(x, axis=-1, eps=1e-5):
    m = x.mean(dim=axis, keepdim=True)
    v = x.var(dim=axis, keepdim=True, unbiased=False)
    return (x - m) / torch.sqrt(v + eps)
def _scatter_add(tgt, idx, src, axis=0):
    return tgt.clone().index_add_(axis, idx, src)
def _segment_sum(src, seg, n):
    out = torch.zeros((n,) + src.shape[1:], dtype=src.dtype)
    return out.index_add_(0, seg, src)
def _pad(x, pw):
    flat = tuple(v for pair in reversed(pw) for v in pair)
    return torch.nn.functional.pad(x, flat)

kernel_name = 'concat_heads__ssm_conv_h3__grouped_kv_mqa__prompt'

def setup_inputs():

    _w = (lambda a: (lambda t: t.to(torch.bfloat16) if t.is_floating_point() else t)(torch.as_tensor(a)))
    def _A_setup():
        B, S, D = 1, 2048, 576
        ks = 4
        return {
            "q": _w((lambda *s: torch.randn(*s, dtype=torch.bfloat16) * 0.05)(B, S, D)),
            "k": _w((lambda *s: torch.randn(*s, dtype=torch.bfloat16) * 0.2)(B, S, D)),
            "v": _w((lambda *s: torch.randn(*s, dtype=torch.bfloat16) * 0.2)(B, S, D)),
            "shift_k": _w((lambda *s: torch.randn(*s, dtype=torch.bfloat16) * 0.2)(D, ks) * 0.5),
            "ssm_k": _w((lambda *s: torch.randn(*s, dtype=torch.bfloat16) * 0.8)(D, S) * (1.0 / S ** 0.5)),
            "ssm_D": _w((lambda *s: torch.randn(*s, dtype=torch.bfloat16) * 0.2)(D)),
        }
    _A_in = _A_setup()

    def _B_setup():
        B, S, D, H = 1, 2048, 576, 32
        Dh = D // H
        return {
            "x": _w((lambda *s: torch.randn(*s, dtype=torch.bfloat16) * 0.05)(B, S, D)),
            "wq": _w((lambda *s: torch.randn(*s, dtype=torch.bfloat16) * 0.8)(D, H * Dh)),
            "wk": _w((lambda *s: torch.randn(*s, dtype=torch.bfloat16) * 0.8)(D, Dh)),
            "wv": _w((lambda *s: torch.randn(*s, dtype=torch.bfloat16) * 0.05)(D, Dh)),
            "wo": _w((lambda *s: torch.randn(*s, dtype=torch.bfloat16) * 0.05)(H * Dh, D)),
        }
    _B_in = _B_setup()

    return {
        "A_q": _A_in["q"],
        "A_k": _A_in["k"],
        "A_v": _A_in["v"],
        "A_shift_k": _A_in["shift_k"],
        "A_ssm_k": _A_in["ssm_k"],
        "A_ssm_D": _A_in["ssm_D"],
        "B_x": _B_in["x"],
        "B_wq": _B_in["wq"],
        "B_wk": _B_in["wk"],
        "B_wv": _B_in["wv"],
        "B_wo": _B_in["wo"],
    }

def reference(A_q, A_k, A_v, A_shift_k, A_ssm_k, A_ssm_D, B_x, B_wq, B_wk, B_wv, B_wo):
    def _A_ref(q, k, v, shift_k, ssm_k, ssm_D):
        B, S, D = q.shape
        ks = shift_k.shape[-1]
        k_pad = _pad(k, ((0, 0), (ks - 1, 0), (0, 0)))
        k_shift = torch.zeros_like(k)
        for t in range(ks):
            k_shift = k_shift + k_pad[:, t:t + S, :] * shift_k[:, ks - 1 - t][None, None, :]
        kv = k_shift * v
        L = 2 * S
        kv_pad = _pad(kv, ((0, 0), (0, S), (0, 0)))
        kf_pad = _pad(ssm_k, ((0, 0), (0, S)))
        kv_f = (lambda x, axes=(-2,-1): torch.fft.rfft2(x.float(), dim=axes))(kv_pad[:, None, :, :], axes=(1, 2))
        kf_f = (lambda x, axes=(-2,-1): torch.fft.rfft2(x.float(), dim=axes))(kf_pad[:, None, :], axes=(1, 2))
        kf_f = (lambda x, *d: x.permute(*d))(kf_f, 1, 2, 0)
        y_f = kv_f * kf_f[None, :, :, :]
        y = (lambda x, s, axes=(-2,-1): torch.fft.irfft2(x, s=s, dim=axes).to(torch.bfloat16))(y_f, (1, L), axes=(1, 2))[:, 0, :S, :]
        y = y + kv * ssm_D[None, None, :]
        return q * y

    def _B_ref(x, wq, wk, wv, wo):
        B, S, D = x.shape
        H = 32
        Dh = D // H
        q = (lambda x, *d: x.permute(*d))((x @ wq).reshape(B, S, H, Dh), 0, 2, 1, 3)
        k = x @ wk
        v = x @ wv
        scores = torch.einsum("bhqd,bkd->bhqk", q, k) * (Dh ** -0.5)
        p = _softmax(scores, axis=-1)
        o = torch.einsum("bhqk,bkd->bhqd", p, v)
        return (lambda x, *d: x.permute(*d))(o, 0, 2, 1, 3).reshape(B, S, H * Dh) @ wo

    a = _A_ref(A_q, A_k, A_v, A_shift_k, A_ssm_k, A_ssm_D)
    b = _B_ref(B_x, B_wq, B_wk, B_wv, B_wo)
    return torch.concatenate([a, b], axis=-1)
```

```cpp
#include <hip/hip_runtime.h>
#include <hip/hip_bf16.h>

typedef unsigned short u16;
typedef unsigned long long u64;
typedef __attribute__((ext_vector_type(16))) float f16v;
typedef __attribute__((ext_vector_type(8))) float f8v;
typedef __attribute__((ext_vector_type(4))) float f4v;
typedef __attribute__((ext_vector_type(2))) float f2v;
typedef __attribute__((ext_vector_type(4))) unsigned u4v;
typedef __attribute__((ext_vector_type(2))) unsigned u2v;
typedef __attribute__((ext_vector_type(8))) __bf16 b8v;
typedef __attribute__((ext_vector_type(2))) __bf16 b2v;
typedef __attribute__((ext_vector_type(4))) short s4v;
#define LAS __attribute__((address_space(3)))
#define GAS __attribute__((address_space(1)))
#define RLX_AGENT __ATOMIC_RELAXED, __HIP_MEMORY_SCOPE_AGENT
#define DI __device__ __forceinline__

constexpr unsigned WS_E = 0, WS_FA = 4096, WS_FO = 20480, WS_FKV = 53248, WS_FY = 86016;
constexpr unsigned WS_KV = 118784;
constexpr unsigned CHB = 19456;
constexpr unsigned WS_XR = WS_KV + 8 * CHB;
constexpr unsigned XRS = 4224;
constexpr unsigned WS_Z = WS_XR + 576 * XRS;
constexpr unsigned WS_O = WS_Z + 2359296;
constexpr unsigned WS_END = WS_O + 2359296;
constexpr unsigned L_X = 0, L_WQ = 73728, L_WKV = 73728 + 6 * 9216;
constexpr unsigned L_CONV = 0, CVS = 25920, HS_ = 4160, XO_ = 16640, XS_ = 4640;
constexpr unsigned L_Z = 6 * CHB, L_WO = 0, L_EXO = 4 * CHB, L_O = 4 * CHB;
constexpr unsigned L_MISC = 156672, L_CRED = L_MISC + 1024, LDS_BYTES = 163840;
constexpr unsigned SY(int i) { return i < 60 ? L_MISC + 16 + 4 * i : L_MISC + 272 + 4 * (i - 60); }
enum { RDYQ = 0, KVDONE = 9, CQ2 = 10, CQ4 = 11, DONEQ = 12, DONEX = 13, KMAXV = 14, KMAXF = 15, RDYC = 16, HS1 = 26, HS2 = 27, HS3 = 28,
       PUBA = 29, PUBY = 30, OPUB = 31, RDYW = 32, KMAXP = 33, HS4 = 35, FKVOK = 36, FYOK = 37, HS5 = 38, TAGW = 39, RDYKV = 40, KVRD = 41,
       PROG = 42, PUBKV = 50, QRDY = 3, OOK = 4, RP = 76, OF = 68 };
#define PPOLL 6
#define CPRIO 2
#define FYDELAY 40
#define FYP 12
#define FKVP 12
#define WPACE 1
#define ODELAY 1
#define FKV_DELAY 60
constexpr unsigned PU = 0xdddd;
constexpr unsigned SPIN_CAP = 1u << 20;

#define TS(n) do {} while (0)
#define TSDUMP() do {} while (0)

DI void dma(const void* base, unsigned voff, unsigned lds) {
  asm volatile("s_mov_b32 m0, %2\n\ts_nop 0\n\tglobal_load_lds_dwordx4 %0, %1" :: "v"(voff), "s"(base), "s"(lds) : "memory");
}
DI void dma_sc1(const void* base, unsigned voff, unsigned lds) {
  asm volatile("s_mov_b32 m0, %2\n\ts_nop 0\n\tglobal_load_lds_dwordx4 %0, %1 sc1" :: "v"(voff), "s"(base), "s"(lds) : "memory");
}
DI void dma4_sc1(const void* base, unsigned voff, unsigned lds) {
  asm volatile("s_mov_b32 m0, %2\n\ts_nop 0\n\tglobal_load_lds_dwordx4 %0, %1 sc1\n\tglobal_load_lds_dwordx4 %0, %1 offset:1024 sc1\n\t"
               "global_load_lds_dwordx4 %0, %1 offset:2048 sc1\n\tglobal_load_lds_dwordx4 %0, %1 offset:3072 sc1"
               :: "v"(voff), "s"(base), "s"(lds) : "memory");
}
DI void dma4(const void* base, unsigned voff, unsigned voff3, unsigned lds) {
  asm volatile("s_mov_b32 m0, %3\n\ts_nop 0\n\tglobal_load_lds_dwordx4 %0, %2\n\tglobal_load_lds_dwordx4 %0, %2 offset:1024\n\t"
               "global_load_lds_dwordx4 %0, %2 offset:2048\n\tglobal_load_lds_dwordx4 %1, %2 offset:3072"
               :: "v"(voff), "v"(voff3), "s"(base), "s"(lds) : "memory");
}
DI void touch(const void* base, unsigned voff, unsigned lds) {
  asm volatile("s_mov_b32 m0, %2\n\ts_nop 0\n\tglobal_load_lds_dword %0, %1" :: "v"(voff), "s"(base), "s"(lds) : "memory");
}
#define VMW_(n) asm volatile("s_waitcnt vmcnt(" #n ")" ::: "memory")
#define VMW(n) VMW_(n)
#define LGW() asm volatile("s_waitcnt lgkmcnt(0)" ::: "memory")
DI unsigned rfl(unsigned v) { return (unsigned)__builtin_amdgcn_readfirstlane((int)v); }
DI unsigned lds_ld(unsigned a) { unsigned v; asm volatile("ds_read_b32 %0, %1\n\ts_waitcnt lgkmcnt(0)" : "=v"(v) : "v"(a) : "memory"); return v; }
DI void lds_st(unsigned a, unsigned v) { asm volatile("ds_write_b32 %0, %1" :: "v"(a), "v"(v) : "memory"); }
DI void sig(unsigned a) {
  asm volatile("s_waitcnt lgkmcnt(0)\n\ts_mov_b64 exec, 1\n\tds_add_u32 %0, %1\n\ts_mov_b64 exec, -1" :: "v"(a), "v"(1u) : "memory");
}
DI void sig_nd(unsigned a) {
  asm volatile("s_mov_b64 exec, 1\n\tds_add_u32 %0, %1\n\ts_mov_b64 exec, -1" :: "v"(a), "v"(1u) : "memory");
}
DI unsigned sig_rtn(unsigned a) {
  unsigned r;
  asm volatile("s_waitcnt lgkmcnt(0)\n\ts_mov_b64 exec, 1\n\tds_add_rtn_u32 %0, %1, %2\n\ts_mov_b64 exec, -1\n\ts_waitcnt lgkmcnt(0)"
               : "=&v"(r) : "v"(a), "v"(1u) : "memory");
  return rfl(r);
}
template <int SL = 1>
DI void wait_ge(unsigned a, unsigned need) {
  _Pragma("unroll 1") for (unsigned s = 0; s < SPIN_CAP; ++s) { if (rfl(lds_ld(a)) >= need) break; __builtin_amdgcn_s_sleep(SL); }
}
template <int SL>
DI void wait_words(unsigned a, int n, unsigned need, int lane) {
  _Pragma("unroll 1") for (unsigned s = 0; s < SPIN_CAP; ++s) {
    unsigned v = lds_ld(a + (lane & (n - 1)) * 4);
    if (__all(v >= need)) break;
    __builtin_amdgcn_s_sleep(SL);
  }
}
DI unsigned opq(unsigned x) { asm volatile("" : "+v"(x)); return x; }
DI int lane_now() { int l; asm volatile("v_mbcnt_lo_u32_b32 %0, -1, 0\n\tv_mbcnt_hi_u32_b32 %0, -1, %0" : "=v"(l)); return l; }
#define SB() __builtin_amdgcn_sched_barrier(0)
DI void barrier() { asm volatile("s_waitcnt lgkmcnt(0)\n\ts_barrier" ::: "memory"); }
DI u4v lr128(unsigned a) { return *(LAS const u4v*)(size_t)a; }
DI u2v lr64(unsigned a) { return *(LAS const u2v*)(size_t)a; }
DI unsigned lr32(unsigned a) { return *(LAS const unsigned*)(size_t)a; }
DI void lw128(unsigned a, u4v v) { *(LAS u4v*)(size_t)a = v; }
DI void lw64(unsigned a, u2v v) { *(LAS u2v*)(size_t)a = v; }
DI u2v ltr(unsigned a) { return __builtin_bit_cast(u2v, __builtin_amdgcn_ds_read_tr16_b64_v4i16((LAS s4v*)(size_t)a)); }
DI u4v ltr2(unsigned a, unsigned b) { u2v x = ltr(a), y = ltr(b); return u4v{x[0], x[1], y[0], y[1]}; }
DI unsigned pk(float lo, float hi) { return __builtin_bit_cast(unsigned, __builtin_convertvector(f2v{lo, hi}, b2v)); }
DI float blo(unsigned u) { return __uint_as_float(u << 16); }
DI float bhi(unsigned u) { return __uint_as_float(u & 0xffff0000u); }
DI float rbf(float x) { return blo(pk(x, 0.f) & 0xffffu); }
DI f16v mf32(u4v a, u4v b, f16v c) { return __builtin_amdgcn_mfma_f32_32x32x16_bf16(__builtin_bit_cast(b8v, a), __builtin_bit_cast(b8v, b), c, 0, 0, 0); }
DI f16v mf32k8(u2v a, u2v b, f16v c) { return __builtin_amdgcn_mfma_f32_32x32x8bf16_1k(__builtin_bit_cast(s4v, a), __builtin_bit_cast(s4v, b), c, 0, 0, 0); }
DI f4v mf16(u4v a, u4v b, f4v c) { return __builtin_amdgcn_mfma_f32_16x16x32_bf16(__builtin_bit_cast(b8v, a), __builtin_bit_cast(b8v, b), c, 0, 0, 0); }
DI unsigned sw32lo(unsigned x) { auto r = __builtin_amdgcn_permlane32_swap(x, x, false, false); return r[0]; }
DI unsigned sw32hi(unsigned x) { auto r = __builtin_amdgcn_permlane32_swap(x, x, false, false); return r[1]; }
DI float xsum32(float x) { auto r = __builtin_amdgcn_permlane32_swap(__float_as_uint(x), __float_as_uint(x), false, false); return __uint_as_float(r[0]) + __uint_as_float(r[1]); }
DI float xmax32(float x) { auto r = __builtin_amdgcn_permlane32_swap(__float_as_uint(x), __float_as_uint(x), false, false); return fmaxf(__uint_as_float(r[0]), __uint_as_float(r[1])); }
DI float xsum16(float x) { auto r = __builtin_amdgcn_permlane16_swap(__float_as_uint(x), __float_as_uint(x), false, false); return __uint_as_float(r[0]) + __uint_as_float(r[1]); }
DI unsigned sw16odd(unsigned x) { auto r = __builtin_amdgcn_permlane16_swap(x, x, false, false); return r[1]; }
template <int C> DI float dppmax(float x) { return fmaxf(x, __int_as_float(__builtin_amdgcn_update_dpp(0, __float_as_int(x), C, 0xf, 0xf, false))); }
DI float rmax16(float x) { return dppmax<0x121>(dppmax<0x122>(dppmax<0x124>(dppmax<0x128>(x)))); }
DI float wmax(float x) {
  x = rmax16(x);
  auto r = __builtin_amdgcn_permlane16_swap(__float_as_uint(x), __float_as_uint(x), false, false);
  return xmax32(fmaxf(__uint_as_float(r[0]), __uint_as_float(r[1])));
}
typedef __amdgpu_buffer_rsrc_t rsrc_t;
DI void st128(rsrc_t r, unsigned off, u4v v) { __builtin_amdgcn_raw_buffer_store_b128(v, r, (int)off, 0, 16); }
DI void st64(rsrc_t r, unsigned off, unsigned soff, u2v v) { __builtin_amdgcn_raw_buffer_store_b64(v, r, (int)off, (int)soff, 16); }
DI float dot2(unsigned a, float c) { return __builtin_amdgcn_fdot2_f32_bf16(__builtin_bit_cast(b2v, a), __builtin_bit_cast(b2v, a), c, false); }
DI void st32(rsrc_t r, unsigned off, unsigned v) { __builtin_amdgcn_raw_buffer_store_b32(v, r, (int)off, 0, 16); }
#define OAUX 0
DI void ost128(rsrc_t r, unsigned eoff, u4v v) { __builtin_amdgcn_raw_buffer_store_b128(v, r, (int)(eoff * 2), 0, OAUX); }
DI void ost64(rsrc_t r, unsigned eoff, u2v v) { __builtin_amdgcn_raw_buffer_store_b64(v, r, (int)(eoff * 2), 0, OAUX); }
DI unsigned gld32(const char* ws, unsigned off) { return __hip_atomic_load((GAS const unsigned*)(ws + off), RLX_AGENT); }
DI u64 gld64(const char* ws, unsigned off) { return __hip_atomic_load((GAS const u64*)(ws + off), RLX_AGENT); }
DI void gst32(char* ws, unsigned off, unsigned v) { __hip_atomic_store((GAS unsigned*)(ws + off), v, RLX_AGENT); }
DI void gst64(char* ws, unsigned off, u64 v) { __hip_atomic_store((GAS u64*)(ws + off), v, RLX_AGENT); }

template <int SL, class SV>
DI void poll(const char* ws, unsigned off, unsigned tag, SV&& between) {
  bool need = true;
  _Pragma("unroll 1") for (unsigned s = 0; s < SPIN_CAP; ++s) {
    if (need) need = gld32(ws, off) != tag;
    if (!__any(need)) break;
    between();
    __builtin_amdgcn_s_sleep(SL);
  }
}
template <int MASK> DI void conv_pro(unsigned fa, unsigned ga, u4v (&F)[8], u4v (&G)[8]);
template <int MASK> DI void conv_res(unsigned fa, unsigned ga, u4v (&F)[8], u4v (&G)[8], f4v (&acc)[8]);
template <> DI void conv_pro<0xc4>(unsigned fa, unsigned ga, u4v (&F)[8], u4v (&G)[8]) {
  G[7] = lr128(ga + 0); F[0] = lr128(fa + 0); G[2] = lr128(ga + 2560); G[6] = lr128(ga + 512); F[4] = lr128(fa + 2048); F[5] = lr128(fa + 2560); F[1] = lr128(fa + 512); G[3] = lr128(ga + 2048); G[1] = lr128(ga + 3072); G[5] = lr128(ga + 1024); F[3] = lr128(fa + 1536); F[6] = lr128(fa + 3072); G[4] = lr128(ga + 1536); F[2] = lr128(fa + 1024); G[0] = lr128(ga + 3584); F[7] = lr128(fa + 3584);
}
template <> DI void conv_res<0xc4>(unsigned fa, unsigned ga, u4v (&F)[8], u4v (&G)[8], f4v (&acc)[8]) {
  acc[7] = mf16(G[7], F[0], acc[7]); SB(); G[7] = lr128(ga + 0); SB();
  acc[2] = mf16(G[2], F[0], acc[2]); SB();
  acc[6] = mf16(G[6], F[0], acc[6]); SB(); F[0] = lr128(fa + 0); SB();
  acc[6] = mf16(G[2], F[4], acc[6]); SB();
  acc[7] = mf16(G[2], F[5], acc[7]); SB(); G[2] = lr128(ga + 2560); SB();
  acc[7] = mf16(G[6], F[1], acc[7]); SB(); G[6] = lr128(ga + 512); SB();
  acc[7] = mf16(G[3], F[4], acc[7]); SB(); F[4] = lr128(fa + 2048); SB();
  acc[6] = mf16(G[1], F[5], acc[6]); SB(); F[5] = lr128(fa + 2560); SB();
  acc[2] = mf16(G[1], F[1], acc[2]); SB();
  acc[6] = mf16(G[5], F[1], acc[6]); SB(); F[1] = lr128(fa + 512); SB();
  acc[6] = mf16(G[3], F[3], acc[6]); SB(); G[3] = lr128(ga + 2048); SB();
  acc[7] = mf16(G[1], F[6], acc[7]); SB(); G[1] = lr128(ga + 3072); SB();
  acc[7] = mf16(G[4], F[3], acc[7]); SB(); F[3] = lr128(fa + 1536); SB();
  acc[7] = mf16(G[5], F[2], acc[7]); SB(); G[5] = lr128(ga + 1024); SB();
  acc[6] = mf16(G[0], F[6], acc[6]); SB(); F[6] = lr128(fa + 3072); SB();
  acc[6] = mf16(G[4], F[2], acc[6]); SB(); G[4] = lr128(ga + 1536); SB();
  acc[2] = mf16(G[0], F[2], acc[2]); SB(); F[2] = lr128(fa + 1024); SB();
  acc[7] = mf16(G[0], F[7], acc[7]); SB(); G[0] = lr128(ga + 3584); F[7] = lr128(fa + 3584); SB();
}
template <> DI void conv_pro<0x3b>(unsigned fa, unsigned ga, u4v (&F)[8], u4v (&G)[8]) {
  G[5] = lr128(ga + 1024); F[0] = lr128(fa + 0); G[4] = lr128(ga + 1536); G[0] = lr128(ga + 3584); F[5] = lr128(fa + 2560); F[1] = lr128(fa + 512); G[3] = lr128(ga + 2048); G[1] = lr128(ga + 3072); F[4] = lr128(fa + 2048); F[3] = lr128(fa + 1536); G[2] = lr128(ga + 2560); F[2] = lr128(fa + 1024);
}
template <> DI void conv_res<0x3b>(unsigned fa, unsigned ga, u4v (&F)[8], u4v (&G)[8], f4v (&acc)[8]) {
  acc[5] = mf16(G[5], F[0], acc[5]); SB(); G[5] = lr128(ga + 1024); SB();
  acc[4] = mf16(G[4], F[0], acc[4]); SB();
  acc[0] = mf16(G[0], F[0], acc[0]); SB();
  acc[5] = mf16(G[0], F[5], acc[5]); SB(); F[5] = lr128(fa + 2560); SB();
  acc[5] = mf16(G[4], F[1], acc[5]); SB(); G[4] = lr128(ga + 1536); SB();
  acc[1] = mf16(G[0], F[1], acc[1]); SB();
  acc[3] = mf16(G[3], F[0], acc[3]); SB();
  acc[1] = mf16(G[1], F[0], acc[1]); SB(); F[0] = lr128(fa + 0); SB();
  acc[4] = mf16(G[0], F[4], acc[4]); SB();
  acc[3] = mf16(G[0], F[3], acc[3]); SB(); G[0] = lr128(ga + 3584); SB();
  acc[4] = mf16(G[3], F[1], acc[4]); SB();
  acc[3] = mf16(G[2], F[1], acc[3]); SB(); F[1] = lr128(fa + 512); SB();
  acc[5] = mf16(G[3], F[2], acc[5]); SB(); G[3] = lr128(ga + 2048); SB();
  acc[5] = mf16(G[1], F[4], acc[5]); SB(); F[4] = lr128(fa + 2048); SB();
  acc[4] = mf16(G[1], F[3], acc[4]); SB();
  acc[3] = mf16(G[1], F[2], acc[3]); SB(); G[1] = lr128(ga + 3072); SB();
  acc[5] = mf16(G[2], F[3], acc[5]); SB(); F[3] = lr128(fa + 1536); SB();
  acc[4] = mf16(G[2], F[2], acc[4]); SB(); G[2] = lr128(ga + 2560); F[2] = lr128(fa + 1024); SB();
}
template <> DI void conv_pro<0x81>(unsigned fa, unsigned ga, u4v (&F)[8], u4v (&G)[8]) {
  G[1] = lr128(ga + 3072); F[6] = lr128(fa + 3072); G[3] = lr128(ga + 2048); F[4] = lr128(fa + 2048); G[4] = lr128(ga + 1536); F[3] = lr128(fa + 1536); G[6] = lr128(ga + 512); F[1] = lr128(fa + 512); G[7] = lr128(ga + 0); F[0] = lr128(fa + 0); G[0] = lr128(ga + 3584); F[7] = lr128(fa + 3584); G[2] = lr128(ga + 2560); F[5] = lr128(fa + 2560); G[5] = lr128(ga + 1024); F[2] = lr128(fa + 1024);
}
template <> DI void conv_res<0x81>(unsigned fa, unsigned ga, u4v (&F)[8], u4v (&G)[8], f4v (&acc)[8]) {
  acc[7] = mf16(G[1], F[6], acc[7]); SB(); G[1] = lr128(ga + 3072); F[6] = lr128(fa + 3072); SB();
  acc[7] = mf16(G[3], F[4], acc[7]); SB(); G[3] = lr128(ga + 2048); F[4] = lr128(fa + 2048); SB();
  acc[7] = mf16(G[4], F[3], acc[7]); SB(); G[4] = lr128(ga + 1536); F[3] = lr128(fa + 1536); SB();
  acc[7] = mf16(G[6], F[1], acc[7]); SB(); G[6] = lr128(ga + 512); F[1] = lr128(fa + 512); SB();
  acc[7] = mf16(G[7], F[0], acc[7]); SB(); G[7] = lr128(ga + 0); SB();
  acc[0] = mf16(G[0], F[0], acc[0]); SB(); F[0] = lr128(fa + 0); SB();
  acc[7] = mf16(G[0], F[7], acc[7]); SB(); G[0] = lr128(ga + 3584); F[7] = lr128(fa + 3584); SB();
  acc[7] = mf16(G[2], F[5], acc[7]); SB(); G[2] = lr128(ga + 2560); F[5] = lr128(fa + 2560); SB();
  acc[7] = mf16(G[5], F[2], acc[7]); SB(); G[5] = lr128(ga + 1024); F[2] = lr128(fa + 1024); SB();
}
template <> DI void conv_pro<0x42>(unsigned fa, unsigned ga, u4v (&F)[8], u4v (&G)[8]) {
  G[0] = lr128(ga + 3584); F[6] = lr128(fa + 3072); F[1] = lr128(fa + 512); G[5] = lr128(ga + 1024); G[6] = lr128(ga + 512); F[0] = lr128(fa + 0); G[1] = lr128(ga + 3072); F[5] = lr128(fa + 2560); G[4] = lr128(ga + 1536); F[2] = lr128(fa + 1024); G[3] = lr128(ga + 2048); F[3] = lr128(fa + 1536); G[2] = lr128(ga + 2560); F[4] = lr128(fa + 2048);
}
template <> DI void conv_res<0x42>(unsigned fa, unsigned ga, u4v (&F)[8], u4v (&G)[8], f4v (&acc)[8]) {
  acc[6] = mf16(G[0], F[6], acc[6]); SB(); F[6] = lr128(fa + 3072); SB();
  acc[1] = mf16(G[0], F[1], acc[1]); SB(); G[0] = lr128(ga + 3584); SB();
  acc[6] = mf16(G[5], F[1], acc[6]); SB(); F[1] = lr128(fa + 512); G[5] = lr128(ga + 1024); SB();
  acc[6] = mf16(G[6], F[0], acc[6]); SB(); G[6] = lr128(ga + 512); SB();
  acc[1] = mf16(G[1], F[0], acc[1]); SB(); F[0] = lr128(fa + 0); SB();
  acc[6] = mf16(G[1], F[5], acc[6]); SB(); G[1] = lr128(ga + 3072); F[5] = lr128(fa + 2560); SB();
  acc[6] = mf16(G[4], F[2], acc[6]); SB(); G[4] = lr128(ga + 1536); F[2] = lr128(fa + 1024); SB();
  acc[6] = mf16(G[3], F[3], acc[6]); SB(); G[3] = lr128(ga + 2048); F[3] = lr128(fa + 1536); SB();
  acc[6] = mf16(G[2], F[4], acc[6]); SB(); G[2] = lr128(ga + 2560); F[4] = lr128(fa + 2048); SB();
}
template <> DI void conv_pro<0x24>(unsigned fa, unsigned ga, u4v (&F)[8], u4v (&G)[8]) {
  G[1] = lr128(ga + 3072); F[4] = lr128(fa + 2048); F[1] = lr128(fa + 512); G[4] = lr128(ga + 1536); G[5] = lr128(ga + 1024); F[0] = lr128(fa + 0); G[2] = lr128(ga + 2560); F[3] = lr128(fa + 1536); G[0] = lr128(ga + 3584); F[5] = lr128(fa + 2560); F[2] = lr128(fa + 1024); G[3] = lr128(ga + 2048);
}
template <> DI void conv_res<0x24>(unsigned fa, unsigned ga, u4v (&F)[8], u4v (&G)[8], f4v (&acc)[8]) {
  acc[5] = mf16(G[1], F[4], acc[5]); SB(); F[4] = lr128(fa + 2048); SB();
  acc[2] = mf16(G[1], F[1], acc[2]); SB(); G[1] = lr128(ga + 3072); SB();
  acc[5] = mf16(G[4], F[1], acc[5]); SB(); F[1] = lr128(fa + 512); G[4] = lr128(ga + 1536); SB();
  acc[5] = mf16(G[5], F[0], acc[5]); SB(); G[5] = lr128(ga + 1024); SB();
  acc[2] = mf16(G[2], F[0], acc[2]); SB(); F[0] = lr128(fa + 0); SB();
  acc[5] = mf16(G[2], F[3], acc[5]); SB(); G[2] = lr128(ga + 2560); F[3] = lr128(fa + 1536); SB();
  acc[5] = mf16(G[0], F[5], acc[5]); SB(); F[5] = lr128(fa + 2560); SB();
  acc[2] = mf16(G[0], F[2], acc[2]); SB(); G[0] = lr128(ga + 3584); SB();
  acc[5] = mf16(G[3], F[2], acc[5]); SB(); F[2] = lr128(fa + 1024); G[3] = lr128(ga + 2048); SB();
}
template <> DI void conv_pro<0x18>(unsigned fa, unsigned ga, u4v (&F)[8], u4v (&G)[8]) {
  G[0] = lr128(ga + 3584); F[4] = lr128(fa + 2048); F[3] = lr128(fa + 1536); G[1] = lr128(ga + 3072); F[2] = lr128(fa + 1024); G[2] = lr128(ga + 2560); F[1] = lr128(fa + 512); G[3] = lr128(ga + 2048); F[0] = lr128(fa + 0); G[4] = lr128(ga + 1536);
}
template <> DI void conv_res<0x18>(unsigned fa, unsigned ga, u4v (&F)[8], u4v (&G)[8], f4v (&acc)[8]) {
  acc[4] = mf16(G[0], F[4], acc[4]); SB(); F[4] = lr128(fa + 2048); SB();
  acc[3] = mf16(G[0], F[3], acc[3]); SB(); G[0] = lr128(ga + 3584); SB();
  acc[4] = mf16(G[1], F[3], acc[4]); SB(); F[3] = lr128(fa + 1536); SB();
  acc[3] = mf16(G[1], F[2], acc[3]); SB(); G[1] = lr128(ga + 3072); SB();
  acc[4] = mf16(G[2], F[2], acc[4]); SB(); F[2] = lr128(fa + 1024); SB();
  acc[3] = mf16(G[2], F[1], acc[3]); SB(); G[2] = lr128(ga + 2560); SB();
  acc[4] = mf16(G[3], F[1], acc[4]); SB(); F[1] = lr128(fa + 512); SB();
  acc[3] = mf16(G[3], F[0], acc[3]); SB(); G[3] = lr128(ga + 2048); SB();
  acc[4] = mf16(G[4], F[0], acc[4]); SB(); F[0] = lr128(fa + 0); G[4] = lr128(ga + 1536); SB();
}
template <int MASK, class SV>
DI void conv_run(unsigned cb, int lane, int r0, int r1, f4v (&acc)[8], SV&& service) {
  const int i = lane & 15, g = lane >> 4;
  unsigned fa = opq(cb + (i & 3) * HS_ + 16 * ((i >> 2) + g) + 64 * r0);
  unsigned ga = opq(cb + XO_ + (i & 1) * XS_ + 448 - 64 * (i >> 1) + 16 * g + 64 * r0);
  u4v F[8], G[8];
  conv_pro<MASK>(fa, ga, F, G);
  _Pragma("unroll 1") for (int r = r0; r < r1; ++r) {
    fa = opq(fa + 64); ga = opq(ga + 64);
    conv_res<MASK>(fa, ga, F, G, acc);
    if (r & 1) service();
  }
}
DI void conv_store(rsrc_t rs, unsigned zoff, int tl, int ch, f4v a) {
  st64(rs, zoff, WS_Z + (unsigned)(4 * tl * 576 + ch) * 128, u2v{pk(a[0], a[1]), pk(a[2], a[3])});
}
template <int MASK, class SV>
DI void conv_full(rsrc_t rs, unsigned cb, int lane, unsigned zoff, int ch, SV&& service) {
  f4v acc[8];
#pragma unroll
  for (int j = 0; j < 8; ++j) acc[j] = f4v{0, 0, 0, 0};
  conv_run<MASK>(cb, lane, 0, 8, acc, service);
#pragma unroll
  for (int tl = 0; tl < 8; ++tl)
    if ((MASK >> tl) & 1) conv_store(rs, zoff, tl, ch, acc[tl]);
}
template <int W>
DI void conv_quarter(unsigned cb, int lane, int h, f4v& lo, f4v& hi) {
  f4v acc[8];
#pragma unroll
  for (int j = 0; j < 8; ++j) acc[j] = f4v{0, 0, 0, 0};
  conv_run<(1 << W) | (1 << (7 - W))>(cb, lane, 2 * h, 2 * h + 2, acc, [] {});
  lo = acc[W]; hi = acc[7 - W];
}

struct KvJob { unsigned kk[11], vv[8]; u4v w; };
DI void kv_read(KvJob& j, unsigned kvt, int pass, int lane) {
  const int sb = lane & 7, cq = 8 * pass + (lane >> 3);
  const unsigned ba = opq(kvt + sb * 144 + cq * 4), ha = ba + (sb ? 39 * 144 : 18432), hs = sb ? 1152u : 144u;
#pragma unroll
  for (int r = 0; r < 3; ++r) j.kk[r] = lr32(ha + r * hs);
#pragma unroll
  for (int r = 0; r < 8; ++r) { j.kk[3 + r] = lr32(ba + r * 1152); j.vv[r] = lr32(ba + 9216 + r * 1152); }
}
DI float fmul1(float a, float b) { float r; asm("v_mul_f32 %0, %1, %2" : "=v"(r) : "v"(a), "v"(b)); return r; }
DI float ffma1(float a, float b, float c) { float r; asm("v_fma_f32 %0, %1, %2, %3" : "=v"(r) : "v"(a), "v"(b), "v"(c)); return r; }
DI float fadd1(float a, float b) { float r; asm("v_add_f32 %0, %1, %2" : "=v"(r) : "v"(a), "v"(b)); return r; }
DI void kv_calc(KvJob& j, bool first, u4v& o0, u4v& o1) {
  if (first) { j.kk[0] = 0; j.kk[1] = 0; j.kk[2] = 0; }
#pragma unroll
  for (int c = 0; c < 2; ++c) {
    const float w0 = blo(j.w[2 * c]), w1 = bhi(j.w[2 * c]), w2 = blo(j.w[2 * c + 1]), w3 = bhi(j.w[2 * c + 1]);
    float k[11], a[8];
#pragma unroll
    for (int r = 0; r < 11; ++r) k[r] = c ? bhi(j.kk[r]) : blo(j.kk[r]);
#pragma unroll
    for (int r = 0; r < 8; ++r)
      a[r] = fmul1(ffma1(k[r + 3], w0, ffma1(k[r + 2], w1, ffma1(k[r + 1], w2, fmul1(k[r], w3)))), c ? bhi(j.vv[r]) : blo(j.vv[r]));
    (c ? o1 : o0) = u4v{pk(a[7], a[6]), pk(a[5], a[4]), pk(a[3], a[2]), pk(a[1], a[0])};
  }
}
DI void kv_store(rsrc_t rs, int t, int dg, int pass, int lane, u4v o0, u4v o1) {
  const int cp = 8 * pass + (lane >> 3), s0 = 64 * t + 8 * (lane & 7);
  const unsigned off = WS_XR + (unsigned)(72 * dg + 2 * cp) * XRS + 2 * (2040 - s0);
  st128(rs, off, o0); st128(rs, off + XRS, o1);
  if (s0 == 0) { st128(rs, off + 16, u4v{0, 0, 0, 0}); st128(rs, off + XRS + 16, u4v{0, 0, 0, 0}); }
}

__global__ void __launch_bounds__(768) fused_h3_mqa(const u16* __restrict__ gq, const u16* __restrict__ gk, const u16* __restrict__ gv,
    const u16* __restrict__ shk, const u16* __restrict__ ssk, const u16* __restrict__ ssd, const u16* __restrict__ gx,
    const u16* __restrict__ wq, const u16* __restrict__ wk, const u16* __restrict__ wv, const u16* __restrict__ wo,
    u16* __restrict__ out, char* ws) {
  extern __shared__ __attribute__((aligned(16))) char smem[];
  unsigned tsv = 0; (void)tsv;
  unsigned tss[32] = {}; (void)tss;
  TS(30);
  const int b = blockIdx.x, lane = threadIdx.x & 63, w = (int)rfl(threadIdx.x >> 6);
  const int t = (b & 7) * 4 + ((b >> 3) & 3), hg = b >> 5, kb = hg >> 1;
  asm volatile("" :: "s"(gx), "s"(wq), "s"(wk), "s"(wv), "s"(ws), "s"(gk), "s"(gv), "s"(shk), "s"(ssk), "s"(ssd), "s"(gq), "s"(wo), "s"(out));
  const rsrc_t rs = __builtin_amdgcn_make_buffer_rsrc(ws, 0, WS_END, 0x00020000);
  const rsrc_t ro = __builtin_amdgcn_make_buffer_rsrc(out, 0, 2048 * 1152 * 2, 0x00020000);
  TS(0);
  u4v gqv[3], gzv[3]; unsigned goo[2];
  const int mt = w & 3, ng = w >> 2;

  auto urgent = [&](int j) {
    if (j < 18) {
      const int rb = 2 * kb + (j & 1), cb = j >> 1, row = 8 * rb + (lane >> 3);
      dma(gx, (unsigned)(((64 * t + row) * 576 + cb * 64 + (((lane & 7) ^ ((row >> 1) & 7)) << 3)) * 2), L_X + (cb * 8 + rb) * 1024);
    } else {
      const int q = 64 * (j - 18) + lane, row = q / 3, pc = q % 3;
      dma((hg & 1) ? wv : wk, (unsigned)((row * 18 + (pc == 2 ? 10 : 8 * pc)) * 2), L_WKV + (j - 18) * 1024);
    }
  };

  if (w == 10) {
    const int B = 4 * t + kb;
    const unsigned cb = WS_KV + (B >> 4) * CHB;
    const unsigned o0 = lane < 16 ? WS_FA + (b & 7) * 2048 + ((b >> 3) & 15) * 128
                      : lane < 32 ? ((hg & 1) ? cb + 9216 + (B & 15) * 640 + 256 : cb + 8192 + ((B >> 2) & 3) * 256 + (B & 1) * 128)
                      : lane < 44 ? ((hg & 1) ? cb + 9216 + (B & 15) * 640 + 512 : WS_FO + t * 1024 + hg * 128)
                      : lane < 48 ? WS_FO + t * 1024 + hg * 128
                      : lane < 52 ? WS_FKV + b * 128
                      : lane < 56 ? WS_FY + b * 128
                                  : WS_O + (unsigned)(2 * t + ((lane >> 2) & 1)) * 36864 + 32768 + hg * 512 + (lane & 3) * 128;
    dma_sc1(ws, o0, L_CRED + 2048);
  }
  if (w == 0) {
    u4v z = {0, 0, 0, 0};
    if ((lane & 15) == 0) { z[0] = 0x3f803f80u; z[1] = 0x00003f80u; }
    if (lane == 6) z[0] = 4u;
    if (lane == 7) z[1] = 4u;
    lw128(L_MISC + lane * 16, z);
  }
  TS(7);
#pragma unroll
  for (int n = 0; n < 4; ++n) { const int j = w + 12 * n; urgent(j < 45 ? j : 44); }
  TS(9);
  const int rot = (hg + 2 * (t & 3)) % 7;
  const unsigned kvt = L_X + rot * 8192;
  struct Slot { unsigned offA, offB; };
  auto wqoff = [&](int q) -> unsigned {
    const int pos = q / 9, row = (pos & 48) | ((pos & 3) << 2) | ((pos >> 2) & 3);
    return (unsigned)((row * 576 + 72 * hg + 8 * (q % 9)) * 2);
  };
  auto mkslot = [&](int sg) -> Slot {
    const int row = 8 * sg + (lane >> 3);
    const unsigned xo = (unsigned)(((64 * t + row) * 576 + (((lane & 7) ^ ((row >> 1) & 7)) << 3)) * 2);
    return Slot{(sg >> 1) == kb ? ((sg & 1) ? 0u : wqoff(512 + lane)) : xo, wqoff(64 * sg + lane)};
  };
  auto issue_a = [&](unsigned offA, int sg, int p) {
    int c = p + rot; c = c >= 9 ? c - 9 : c;
    const bool wq8 = (sg >> 1) == kb;
    dma(wq8 ? (const char*)wq + c * 73728 : (const char*)gx + c * 128, offA,
        wq8 ? ((sg & 1) ? L_CRED + 4096 : L_WQ + 8192 + p * 9216) : L_X + sg * 1024 + c * 8192);
  };
  auto issue_b = [&](unsigned offB, int sg, int p) {
    int c = p + rot; c = c >= 9 ? c - 9 : c;
    dma((const char*)wq + c * 73728, offB, L_WQ + p * 9216 + sg * 1024);
  };
  auto issue_s = [&](const Slot& sl, int sg, int p) { issue_a(sl.offA, sg, p); issue_b(sl.offB, sg, p); };

  const unsigned l16 = lane * 16;
  auto chunk = [&](int c, int slot) {
    const int p0 = (w & 3) < 3 ? 5 * (w & 3) : 14;
    const char* src = ws + WS_KV + c * CHB + p0 * 1024;
    dma4_sc1(src, l16, slot * CHB + p0 * 1024);
    dma_sc1(src + 4096, l16, slot * CHB + p0 * 1024 + 4096);
  };

  if (w < 8) {
    const int hl = w & 3, kh = w >> 2, r = lane & 31, hf = lane >> 5;
    const Slot sl = mkslot(w);
    barrier();
    issue_s(sl, w, 0); issue_s(sl, w, 1);
    VMW(4);
    barrier();
    TS(15);
    const unsigned fa_ = lane < 9 ? SY(RP) + lane * 4 : SY(CQ2);
    const unsigned fn_ = lane < 2 ? 8u : lane < 9 ? 6u : 4u;
    unsigned fs_ = 0;
    auto need = [&](unsigned m) {
      _Pragma("unroll 1") for (unsigned s = 0; s < SPIN_CAP && (fs_ & m) != m; ++s) fs_ = (unsigned)__ballot(lds_ld(fa_) >= fn_);
    };
    const float cs = 0.34004648f;
    auto qrow = [&](int row) -> unsigned { return row < 26 ? L_WQ + row * 152 : L_CRED + (row - 26) * 152; };
    const int s0 = kh ? 5 : 4;
    f16v O0, O1;
    unsigned ka = s0 * CHB + lane * 16, kan = 6 * CHB + lane * 16;
    unsigned kra = hf ? L_MISC : s0 * CHB + 8192 + r * 8, krn = hf ? L_MISC : 6 * CHB + 8192 + r * 8, krm = hf ? L_MISC : 7 * CHB + 8192 + r * 8;
    const unsigned vo = 9216 + hf * 320 + (r < 19 ? r : 19) * 16;
    unsigned va = s0 * CHB + vo, van = 6 * CHB + vo;
    unsigned flg = SY(RDYC + 5 * kh + 1), prg = SY(PROG + w), idx;
    asm volatile(
    "v_mov_b32 v0, 0\n"
    "v_mov_b32 v1, 0\n"
    "v_mov_b32 v2, 0\n"
    "v_mov_b32 v3, 0\n"
    "v_mov_b32 v4, 0\n"
    "v_mov_b32 v5, 0\n"
    "v_mov_b32 v6, 0\n"
    "v_mov_b32 v7, 0\n"
    "v_mov_b32 v8, 0\n"
    "v_mov_b32 v9, 0\n"
    "v_mov_b32 v10, 0\n"
    "v_mov_b32 v11, 0\n"
    "v_mov_b32 v12, 0\n"
    "v_mov_b32 v13, 0\n"
    "v_mov_b32 v14, 0\n"
    "v_mov_b32 v15, 0\n"
    "v_mov_b32 v16, 0\n"
    "v_mov_b32 v17, 0\n"
    "v_mov_b32 v18, 0\n"
    "v_mov_b32 v19, 0\n"
    "v_mov_b32 v20, 0\n"
    "v_mov_b32 v21, 0\n"
    "v_mov_b32 v22, 0\n"
    "v_mov_b32 v23, 0\n"
    "v_mov_b32 v24, 0\n"
    "v_mov_b32 v25, 0\n"
    "v_mov_b32 v26, 0\n"
    "v_mov_b32 v27, 0\n"
    "v_mov_b32 v28, 0\n"
    "v_mov_b32 v29, 0\n"
    "v_mov_b32 v30, 0\n"
    "v_mov_b32 v31, 0\n"
    "v_mov_b32 v112, 0xdddd\n"
        : "={v[0:15]}"(O0), "={v[16:31]}"(O1), "={v112}"(idx),
          "+{v113}"(ka), "+{v114}"(kra), "+{v115}"(krn), "+{v116}"(va), "+{v117}"(flg), "+{v150}"(kan), "+{v151}"(van),
          "+{v119}"(prg), "+{v121}"(krm));
    unsigned exa0 = L_EXO + (unsigned)(hl * 4096 + lane * 16), exb0 = (hl < 3 ? L_EXO + 16384u + hl * 1024 : 8 * CHB) + r * 16;
    unsigned obm = WS_O + (unsigned)(2 * t) * 36864 + (4 * hg + hl) * 1024 + lane * 16;
    unsigned obr = WS_O + (unsigned)(2 * t) * 36864 + (32 + (hg >> 1)) * 1024 + (hg & 1) * 512 + r * 16 + hl * 4;
    asm volatile("" : "+v"(exa0), "+v"(exb0), "+v"(obm), "+v"(obr));
    const unsigned qo0 = opq(qrow(r) + 36 * hl + 8 * hf), qo1 = opq(qrow(32 + r) + 36 * hl + 8 * hf);
    const unsigned sw = opq(lane == 0 ? SY(QRDY) : lane == 1 ? flg - 4 : SY(KMAXV)), sn = lane < 2 ? 4u : 1u;
    if (kh == 0) {
      __builtin_amdgcn_s_setprio(3);
      const int hp = w & 1, rh = w >> 1, i = lane & 15, g = lane >> 4, row = 16 * w + i;
      f16v qa = {};
      f4v qb = {0, 0, 0, 0};
      const unsigned wa0 = L_WQ + (unsigned)(((2 * hf + 4 * ((lane >> 2) & 3)) * 72 + 36 * hp + (lane & 16) + 4 * (lane & 3)) * 2);
      const unsigned wc0 = L_WQ + (unsigned)(((16 * (g >> 1) + 2 * (g & 1) + 4 * ((lane >> 2) & 3)) * 72 + 36 * (lane & 1) + 32) * 2);
      const unsigned xb = L_X + rh * 4096 + (r >> 3) * 1024 + (r & 7) * 128, yb = L_X + (row >> 3) * 1024 + (row & 7) * 128;
      unsigned xa[4], ya[2];
#pragma unroll
      for (int e = 0; e < 4; ++e) xa[e] = xb + (((2 * e + hf) ^ ((r >> 1) & 7)) << 4);
#pragma unroll
      for (int e = 0; e < 2; ++e) ya[e] = yb + (((4 * e + g) ^ ((row >> 1) & 7)) << 4);
      u4v af[9][4], bf[9][4], cf[9][2], df[9][2];
      const unsigned wab[3] = {opq(wa0), opq(wa0 + 27648), opq(wa0 + 55296)}, wcb[3] = {opq(wc0), opq(wc0 + 27648), opq(wc0 + 55296)};
      auto ld = [&](int p, int e) {
        int c = p + rot; c = c >= 9 ? c - 9 : c;
        if (e < 4) {
          af[p][e] = ltr2(wab[p / 3] + (p % 3) * 9216 + e * 2304, wab[p / 3] + (p % 3) * 9216 + e * 2304 + 144);
          bf[p][e] = lr128(xa[e] + c * 8192);
        } else {
          cf[p][e - 4] = ltr2(wcb[p / 3] + (p % 3) * 9216 + (e - 4) * 4608, wcb[p / 3] + (p % 3) * 9216 + (e - 4) * 4608 + 144);
          df[p][e - 4] = lr128(ya[e - 4] + c * 8192);
        }
        SB();
      };
      const unsigned qo = opq(qrow(32 * rh + r) + (36 * hp + 4 * hf) * 2), qp = opq(qrow(row) + (36 * (g & 1) + 32) * 2);
      VMW(2); sig(SY(RP + 0)); VMW(0); sig(SY(RP + 1));
      need(1);
      TS(8);
#pragma unroll
      for (int e = 0; e < 6; ++e) ld(0, e);
#pragma unroll
      for (int p = 0; p < 9; ++p) {
        if (p < 8) need(2u << p);
        if (p == 2) TS(1);
        if (p == 7) TS(2);
#pragma unroll
        for (int e = 0; e < 6; ++e) {
          if (e < 4) qa = mf32(af[p][e], bf[p][e], qa); else qb = mf16(cf[p][e - 4], df[p][e - 4], qb);
          SB();
          if (p < 8) ld(p + 1, e);
        }
        if (p == 1) sig_nd(SY(CQ2));
        if (p == 4) sig_nd(SY(CQ4));
      }
      need(0x200);
#pragma unroll
      for (int j = 0; j < 4; ++j)
        lw64(qo + 16 * j, u2v{pk(qa[4 * j] * cs, qa[4 * j + 1] * cs), pk(qa[4 * j + 2] * cs, qa[4 * j + 3] * cs)});
      if (g < 2) lw64(qp, u2v{pk(qb[0] * cs, qb[1] * cs), pk(qb[2] * cs, qb[3] * cs)});
      sig_nd(SY(QRDY));
    } else {
      const unsigned offx = mkslot(hl).offA;
#pragma unroll
      for (int p = 2; p < 9; ++p) {
        if (p == 6) wait_ge(SY(KVDONE), 2);
        issue_s(sl, w, p); issue_a(offx, hl, p);
        if (p == 3) { VMW(8); sig(SY(RP + 0)); }
        if (p == 4) { VMW(9); sig(SY(RP + 1)); }
        if (p >= 5) { VMW(9); sig(SY(RP + p - 3)); }
      }
      TS(8);
      VMW(6); sig(SY(RP + 6));
      VMW(3); sig(SY(RP + 7));
      VMW(0); sig(SY(RP + 8));
      TS(16);
      __builtin_amdgcn_s_setprio(1);
    }
    TS(3);
    u4v QM[2]; unsigned QR[2]; float qn[2];
    unsigned st = 0, sv = 0;
    _Pragma("unroll 1") for (unsigned s = 0; s < SPIN_CAP && (st & 5) != 5; ++s) { sv = lds_ld(sw); st = (unsigned)__ballot(sv >= sn); }
    const float kmax2 = __uint_as_float((unsigned)__builtin_amdgcn_readlane((int)sv, 2));
#pragma unroll
    for (int c = 0; c < 2; ++c) {
      const unsigned qo = c ? qo1 : qo0;
      QM[c] = u4v{lr32(qo), lr32(qo + 4), lr32(qo + 16), lr32(qo + 20)};
      QR[c] = lr32(qo + 32);
    }
    SB();
#pragma unroll
    for (int c = 0; c < 2; ++c)
      qn[c] = xsum32(dot2(QM[c][0], dot2(QM[c][1], 0.f)) + dot2(QM[c][2], dot2(QM[c][3], dot2(hf ? 0u : QR[c], 0.f))));
    TS(4);
    float mm[2];
    mm[0] = __builtin_amdgcn_sqrtf(qn[0] * kmax2) * 0.51f; mm[1] = __builtin_amdgcn_sqrtf(qn[1] * kmax2) * 0.51f;
    if (!__all(mm[0] <= 35.f && mm[1] <= 35.f)) {
      float m0 = -3.0e38f, m1 = -3.0e38f;
      const u2v r0 = {hf ? 0u : QR[0], 0u}, r1 = {hf ? 0u : QR[1], 0u};
      _Pragma("unroll 1") for (int kt = 0; kt < 32; ++kt) {
        const unsigned cb = WS_KV + (4 * kh + (kt >> 3)) * CHB;
        const u4v kf = __builtin_amdgcn_raw_buffer_load_b128(rs, (int)(cb + (kt & 7) * 1024 + lane * 16), 0, 16);
        const u2v kr = {__builtin_amdgcn_raw_buffer_load_b32(rs, (int)(cb + 8192 + ((kt & 7) >> 1) * 256 + r * 8 + (kt & 1) * 4), 0, 16), 0u};
        f16v s0 = mf32k8(kr, r0, mf32(kf, QM[0], f16v{})), s1 = mf32k8(kr, r1, mf32(kf, QM[1], f16v{}));
#pragma unroll
        for (int j = 0; j < 16; ++j) { m0 = fmaxf(m0, s0[j]); m1 = fmaxf(m1, s1[j]); }
      }
      mm[0] = xmax32(m0); mm[1] = xmax32(m1);
    }
    u2v QA[2], QB[2];
#pragma unroll
    for (int c = 0; c < 2; ++c) {
      const float nm = -mm[c];
      const float b0 = bhi(__float_as_uint(nm)), t1 = nm - b0, b1 = bhi(__float_as_uint(t1)), b2 = bhi(__float_as_uint(t1 - b1));
      mm[c] = -(b0 + b1 + b2);
      const unsigned u0 = (__float_as_uint(b0) >> 16) | __float_as_uint(b1), u1 = __float_as_uint(b2) >> 16;
      QA[c] = u2v{hf ? u0 : QR[c], hf ? u1 : 0u};
      QB[c] = u2v{hf ? u0 : 0u, hf ? u1 : QR[c]};
    }
    {
      asm volatile("" : "+{v[100:101]}"(QA[0]), "+{v[102:103]}"(QB[0]), "+{v[108:109]}"(QA[1]), "+{v[110:111]}"(QB[1]), "+v"(mm[0]), "+v"(mm[1]));
      asm volatile("s_mov_b64 exec, 1\n\tds_add_u32 %0, %1\n\ts_mov_b64 exec, -1" :: "v"(SY(DONEX)), "v"(1u) : "memory");
      if (!(st & 2)) wait_ge(flg - 4, 4);
      __builtin_amdgcn_s_setprio(0);
      TS(5);
      asm volatile(
    "ds_read_b128 v[122:125], v113 offset:0\n"
    "ds_read_b64 v[130:131], v114 offset:0\n"
    "ds_read_b128 v[126:129], v113 offset:1024\n"
    "ds_read_b128 v[134:137], v116 offset:0\n"
    "ds_read_b128 v[138:141], v116 offset:640\n"
    "s_waitcnt lgkmcnt(3)\n"
    "v_mfma_f32_32x32x16_bf16 v[32:47], v[122:125], v[96:99], 0\n"
    "v_mfma_f32_32x32x16_bf16 v[64:79], v[122:125], v[104:107], 0\n"
    "v_mfma_f32_32x32x8_bf16 v[32:47], v[130:131], v[100:101], v[32:47]\n"
    "v_mfma_f32_32x32x8_bf16 v[64:79], v[130:131], v[108:109], v[64:79]\n"
    "ds_read_b128 v[122:125], v113 offset:2048\n"
    "ds_read_b64 v[132:133], v114 offset:256\n"
    "ds_read_b128 v[142:145], v116 offset:1280\n"
    "s_nop 9\n"
    "v_exp_f32 v32, v32\n"
    "v_exp_f32 v33, v33\n"
    "ds_read_b128 v[146:149], v116 offset:1920\n"
    "v_exp_f32 v34, v34\n"
    "v_exp_f32 v35, v35\n"
    "v_exp_f32 v36, v36\n"
    "v_exp_f32 v37, v37\n"
    "s_waitcnt lgkmcnt(4)\n"
    "v_mfma_f32_32x32x16_bf16 v[48:63], v[126:129], v[96:99], 0\n"
    "v_exp_f32 v38, v38\n"
    "v_exp_f32 v39, v39\n"
    "v_exp_f32 v40, v40\n"
    "v_exp_f32 v41, v41\n"
    "v_smfmac_f32_32x32x32_bf16 v[0:15], v[134:137], v[32:39], v112\n"
    "v_exp_f32 v42, v42\n"
    "v_exp_f32 v43, v43\n"
    "v_exp_f32 v44, v44\n"
    "v_exp_f32 v45, v45\n"
    "v_mfma_f32_32x32x8_bf16 v[48:63], v[130:131], v[102:103], v[48:63]\n"
    "v_exp_f32 v46, v46\n"
    "v_exp_f32 v47, v47\n"
    "v_exp_f32 v64, v64\n"
    "v_exp_f32 v65, v65\n"
    "v_smfmac_f32_32x32x32_bf16 v[0:15], v[138:141], v[40:47], v112\n"
    "v_exp_f32 v66, v66\n"
    "v_exp_f32 v67, v67\n"
    "v_exp_f32 v68, v68\n"
    "v_exp_f32 v69, v69\n"
    "v_mfma_f32_32x32x16_bf16 v[80:95], v[126:129], v[104:107], 0\n"
    "ds_read_b128 v[126:129], v113 offset:3072\n"
    "v_exp_f32 v70, v70\n"
    "v_exp_f32 v71, v71\n"
    "v_exp_f32 v72, v72\n"
    "v_exp_f32 v73, v73\n"
    "v_smfmac_f32_32x32x32_bf16 v[16:31], v[134:137], v[64:71], v112\n"
    "ds_read_b128 v[134:137], v116 offset:2560\n"
    "v_exp_f32 v74, v74\n"
    "v_exp_f32 v75, v75\n"
    "v_exp_f32 v76, v76\n"
    "v_exp_f32 v77, v77\n"
    "v_mfma_f32_32x32x8_bf16 v[80:95], v[130:131], v[110:111], v[80:95]\n"
    "ds_read_b64 v[130:131], v114 offset:512\n"
    "v_exp_f32 v78, v78\n"
    "v_exp_f32 v79, v79\n"
    "v_exp_f32 v48, v48\n"
    "v_exp_f32 v49, v49\n"
    "v_smfmac_f32_32x32x32_bf16 v[16:31], v[138:141], v[72:79], v112\n"
    "ds_read_b128 v[138:141], v116 offset:3200\n"
    "v_exp_f32 v50, v50\n"
    "v_exp_f32 v51, v51\n"
    "v_exp_f32 v52, v52\n"
    "v_exp_f32 v53, v53\n"
    "s_waitcnt lgkmcnt(4)\n"
    "v_mfma_f32_32x32x16_bf16 v[32:47], v[122:125], v[96:99], 0\n"
    "v_exp_f32 v54, v54\n"
    "v_exp_f32 v55, v55\n"
    "v_exp_f32 v56, v56\n"
    "v_exp_f32 v57, v57\n"
    "v_smfmac_f32_32x32x32_bf16 v[0:15], v[142:145], v[48:55], v112\n"
    "v_exp_f32 v58, v58\n"
    "v_exp_f32 v59, v59\n"
    "v_exp_f32 v60, v60\n"
    "v_exp_f32 v61, v61\n"
    "v_mfma_f32_32x32x8_bf16 v[32:47], v[132:133], v[100:101], v[32:47]\n"
    "v_exp_f32 v62, v62\n"
    "v_exp_f32 v63, v63\n"
    "v_exp_f32 v80, v80\n"
    "v_exp_f32 v81, v81\n"
    "v_smfmac_f32_32x32x32_bf16 v[0:15], v[146:149], v[56:63], v112\n"
    "v_exp_f32 v82, v82\n"
    "v_exp_f32 v83, v83\n"
    "v_exp_f32 v84, v84\n"
    "v_exp_f32 v85, v85\n"
    "v_mfma_f32_32x32x16_bf16 v[64:79], v[122:125], v[104:107], 0\n"
    "ds_read_b128 v[122:125], v113 offset:4096\n"
    "v_exp_f32 v86, v86\n"
    "v_exp_f32 v87, v87\n"
    "v_exp_f32 v88, v88\n"
    "v_exp_f32 v89, v89\n"
    "v_smfmac_f32_32x32x32_bf16 v[16:31], v[142:145], v[80:87], v112\n"
    "ds_read_b128 v[142:145], v116 offset:3840\n"
    "v_exp_f32 v90, v90\n"
    "v_exp_f32 v91, v91\n"
    "v_exp_f32 v92, v92\n"
    "v_exp_f32 v93, v93\n"
    "v_mfma_f32_32x32x8_bf16 v[64:79], v[132:133], v[108:109], v[64:79]\n"
    "v_exp_f32 v94, v94\n"
    "v_exp_f32 v95, v95\n"
    "v_exp_f32 v32, v32\n"
    "v_exp_f32 v33, v33\n"
    "v_smfmac_f32_32x32x32_bf16 v[16:31], v[146:149], v[88:95], v112\n"
    "ds_read_b128 v[146:149], v116 offset:4480\n"
    "v_exp_f32 v34, v34\n"
    "v_exp_f32 v35, v35\n"
    "v_exp_f32 v36, v36\n"
    "v_exp_f32 v37, v37\n"
    "s_waitcnt lgkmcnt(3)\n"
    "v_mfma_f32_32x32x16_bf16 v[48:63], v[126:129], v[96:99], 0\n"
    "v_exp_f32 v38, v38\n"
    "v_exp_f32 v39, v39\n"
    "v_exp_f32 v40, v40\n"
    "v_exp_f32 v41, v41\n"
    "v_smfmac_f32_32x32x32_bf16 v[0:15], v[134:137], v[32:39], v112\n"
    "v_exp_f32 v42, v42\n"
    "v_exp_f32 v43, v43\n"
    "v_exp_f32 v44, v44\n"
    "v_exp_f32 v45, v45\n"
    "v_mfma_f32_32x32x8_bf16 v[48:63], v[132:133], v[102:103], v[48:63]\n"
    "v_exp_f32 v46, v46\n"
    "v_exp_f32 v47, v47\n"
    "v_exp_f32 v64, v64\n"
    "v_exp_f32 v65, v65\n"
    "v_smfmac_f32_32x32x32_bf16 v[0:15], v[138:141], v[40:47], v112\n"
    "v_exp_f32 v66, v66\n"
    "v_exp_f32 v67, v67\n"
    "v_exp_f32 v68, v68\n"
    "v_exp_f32 v69, v69\n"
    "v_mfma_f32_32x32x16_bf16 v[80:95], v[126:129], v[104:107], 0\n"
    "ds_read_b128 v[126:129], v113 offset:5120\n"
    "v_exp_f32 v70, v70\n"
    "v_exp_f32 v71, v71\n"
    "v_exp_f32 v72, v72\n"
    "v_exp_f32 v73, v73\n"
    "v_smfmac_f32_32x32x32_bf16 v[16:31], v[134:137], v[64:71], v112\n"
    "ds_read_b128 v[134:137], v116 offset:5120\n"
    "v_exp_f32 v74, v74\n"
    "v_exp_f32 v75, v75\n"
    "v_exp_f32 v76, v76\n"
    "v_exp_f32 v77, v77\n"
    "v_mfma_f32_32x32x8_bf16 v[80:95], v[132:133], v[110:111], v[80:95]\n"
    "ds_read_b64 v[132:133], v114 offset:768\n"
    "v_exp_f32 v78, v78\n"
    "v_exp_f32 v79, v79\n"
    "v_exp_f32 v48, v48\n"
    "v_exp_f32 v49, v49\n"
    "v_smfmac_f32_32x32x32_bf16 v[16:31], v[138:141], v[72:79], v112\n"
    "ds_read_b128 v[138:141], v116 offset:5760\n"
    "v_exp_f32 v50, v50\n"
    "v_exp_f32 v51, v51\n"
    "v_exp_f32 v52, v52\n"
    "v_exp_f32 v53, v53\n"
    "s_waitcnt lgkmcnt(4)\n"
    "v_mfma_f32_32x32x16_bf16 v[32:47], v[122:125], v[96:99], 0\n"
    "v_exp_f32 v54, v54\n"
    "v_exp_f32 v55, v55\n"
    "v_exp_f32 v56, v56\n"
    "v_exp_f32 v57, v57\n"
    "v_smfmac_f32_32x32x32_bf16 v[0:15], v[142:145], v[48:55], v112\n"
    "v_exp_f32 v58, v58\n"
    "v_exp_f32 v59, v59\n"
    "v_exp_f32 v60, v60\n"
    "v_exp_f32 v61, v61\n"
    "v_mfma_f32_32x32x8_bf16 v[32:47], v[130:131], v[100:101], v[32:47]\n"
    "v_exp_f32 v62, v62\n"
    "v_exp_f32 v63, v63\n"
    "v_exp_f32 v80, v80\n"
    "v_exp_f32 v81, v81\n"
    "v_smfmac_f32_32x32x32_bf16 v[0:15], v[146:149], v[56:63], v112\n"
    "v_exp_f32 v82, v82\n"
    "v_exp_f32 v83, v83\n"
    "v_exp_f32 v84, v84\n"
    "v_exp_f32 v85, v85\n"
    "v_mfma_f32_32x32x16_bf16 v[64:79], v[122:125], v[104:107], 0\n"
    "ds_read_b128 v[122:125], v113 offset:6144\n"
    "v_exp_f32 v86, v86\n"
    "v_exp_f32 v87, v87\n"
    "v_exp_f32 v88, v88\n"
    "v_exp_f32 v89, v89\n"
    "v_smfmac_f32_32x32x32_bf16 v[16:31], v[142:145], v[80:87], v112\n"
    "ds_read_b128 v[142:145], v116 offset:6400\n"
    "v_exp_f32 v90, v90\n"
    "v_exp_f32 v91, v91\n"
    "v_exp_f32 v92, v92\n"
    "v_exp_f32 v93, v93\n"
    "v_mfma_f32_32x32x8_bf16 v[64:79], v[130:131], v[108:109], v[64:79]\n"
    "v_exp_f32 v94, v94\n"
    "v_exp_f32 v95, v95\n"
    "v_exp_f32 v32, v32\n"
    "v_exp_f32 v33, v33\n"
    "v_smfmac_f32_32x32x32_bf16 v[16:31], v[146:149], v[88:95], v112\n"
    "ds_read_b128 v[146:149], v116 offset:7040\n"
    "v_exp_f32 v34, v34\n"
    "v_exp_f32 v35, v35\n"
    "v_exp_f32 v36, v36\n"
    "v_exp_f32 v37, v37\n"
    "s_waitcnt lgkmcnt(3)\n"
    "v_mfma_f32_32x32x16_bf16 v[48:63], v[126:129], v[96:99], 0\n"
    "v_exp_f32 v38, v38\n"
    "v_exp_f32 v39, v39\n"
    "v_exp_f32 v40, v40\n"
    "v_exp_f32 v41, v41\n"
    "v_smfmac_f32_32x32x32_bf16 v[0:15], v[134:137], v[32:39], v112\n"
    "v_exp_f32 v42, v42\n"
    "v_exp_f32 v43, v43\n"
    "v_exp_f32 v44, v44\n"
    "v_exp_f32 v45, v45\n"
    "v_mfma_f32_32x32x8_bf16 v[48:63], v[130:131], v[102:103], v[48:63]\n"
    "v_exp_f32 v46, v46\n"
    "v_exp_f32 v47, v47\n"
    "v_exp_f32 v64, v64\n"
    "v_exp_f32 v65, v65\n"
    "v_smfmac_f32_32x32x32_bf16 v[0:15], v[138:141], v[40:47], v112\n"
    "v_exp_f32 v66, v66\n"
    "v_exp_f32 v67, v67\n"
    "v_exp_f32 v68, v68\n"
    "v_exp_f32 v69, v69\n"
    "v_mfma_f32_32x32x16_bf16 v[80:95], v[126:129], v[104:107], 0\n"
    "ds_read_b128 v[126:129], v113 offset:7168\n"
    "v_exp_f32 v70, v70\n"
    "v_exp_f32 v71, v71\n"
    "v_exp_f32 v72, v72\n"
    "v_exp_f32 v73, v73\n"
    "v_smfmac_f32_32x32x32_bf16 v[16:31], v[134:137], v[64:71], v112\n"
    "ds_read_b128 v[134:137], v116 offset:7680\n"
    "v_exp_f32 v74, v74\n"
    "v_exp_f32 v75, v75\n"
    "v_exp_f32 v76, v76\n"
    "v_exp_f32 v77, v77\n"
    "v_mfma_f32_32x32x8_bf16 v[80:95], v[130:131], v[110:111], v[80:95]\n"
    "v_exp_f32 v78, v78\n"
    "v_exp_f32 v79, v79\n"
    "ds_read_b32 v118, v117 offset:0\n"
    "v_exp_f32 v48, v48\n"
    "v_exp_f32 v49, v49\n"
    "v_smfmac_f32_32x32x32_bf16 v[16:31], v[138:141], v[72:79], v112\n"
    "ds_read_b128 v[138:141], v116 offset:8320\n"
    "v_exp_f32 v50, v50\n"
    "v_exp_f32 v51, v51\n"
    "v_exp_f32 v52, v52\n"
    "v_exp_f32 v53, v53\n"
    "s_waitcnt lgkmcnt(4)\n"
    "v_mfma_f32_32x32x16_bf16 v[32:47], v[122:125], v[96:99], 0\n"
    "v_exp_f32 v54, v54\n"
    "v_exp_f32 v55, v55\n"
    "v_exp_f32 v56, v56\n"
    "v_exp_f32 v57, v57\n"
    "v_smfmac_f32_32x32x32_bf16 v[0:15], v[142:145], v[48:55], v112\n"
    "v_exp_f32 v58, v58\n"
    "v_exp_f32 v59, v59\n"
    "v_exp_f32 v60, v60\n"
    "v_exp_f32 v61, v61\n"
    "v_mfma_f32_32x32x8_bf16 v[32:47], v[132:133], v[100:101], v[32:47]\n"
    "v_exp_f32 v62, v62\n"
    "v_exp_f32 v63, v63\n"
    "v_exp_f32 v80, v80\n"
    "v_exp_f32 v81, v81\n"
    "v_smfmac_f32_32x32x32_bf16 v[0:15], v[146:149], v[56:63], v112\n"
    "v_exp_f32 v82, v82\n"
    "v_exp_f32 v83, v83\n"
    "v_exp_f32 v84, v84\n"
    "v_exp_f32 v85, v85\n"
    "v_mfma_f32_32x32x16_bf16 v[64:79], v[122:125], v[104:107], 0\n"
    "v_exp_f32 v86, v86\n"
    "v_exp_f32 v87, v87\n"
    "v_exp_f32 v88, v88\n"
    "v_exp_f32 v89, v89\n"
    "v_smfmac_f32_32x32x32_bf16 v[16:31], v[142:145], v[80:87], v112\n"
    "ds_read_b128 v[142:145], v116 offset:8960\n"
    "v_exp_f32 v90, v90\n"
    "v_exp_f32 v91, v91\n"
    "v_exp_f32 v92, v92\n"
    "v_exp_f32 v93, v93\n"
    "v_mfma_f32_32x32x8_bf16 v[64:79], v[132:133], v[108:109], v[64:79]\n"
    "v_exp_f32 v94, v94\n"
    "v_exp_f32 v95, v95\n"
    "v_exp_f32 v32, v32\n"
    "v_exp_f32 v33, v33\n"
    "v_smfmac_f32_32x32x32_bf16 v[16:31], v[146:149], v[88:95], v112\n"
    "ds_read_b128 v[146:149], v116 offset:9600\n"
    "v_exp_f32 v34, v34\n"
    "v_exp_f32 v35, v35\n"
    "v_exp_f32 v36, v36\n"
    "v_exp_f32 v37, v37\n"
    "s_waitcnt lgkmcnt(2)\n"
    "v_mfma_f32_32x32x16_bf16 v[48:63], v[126:129], v[96:99], 0\n"
    "v_readfirstlane_b32 s41, v118\n"
    "v_exp_f32 v38, v38\n"
    "v_exp_f32 v39, v39\n"
    "v_exp_f32 v40, v40\n"
    "v_exp_f32 v41, v41\n"
    "v_smfmac_f32_32x32x32_bf16 v[0:15], v[134:137], v[32:39], v112\n"
    "s_cmp_ge_u32 s41, 4\n"
    "s_cbranch_scc0 30f\n"
    "20:\n"
    "v_exp_f32 v42, v42\n"
    "ds_read_b128 v[122:125], v150 offset:0\n"
    "ds_read_b64 v[130:131], v115 offset:0\n"
    "v_exp_f32 v43, v43\n"
    "v_exp_f32 v44, v44\n"
    "v_exp_f32 v45, v45\n"
    "v_mfma_f32_32x32x8_bf16 v[48:63], v[132:133], v[102:103], v[48:63]\n"
    "v_exp_f32 v46, v46\n"
    "v_exp_f32 v47, v47\n"
    "v_exp_f32 v64, v64\n"
    "v_exp_f32 v65, v65\n"
    "v_smfmac_f32_32x32x32_bf16 v[0:15], v[138:141], v[40:47], v112\n"
    "v_exp_f32 v66, v66\n"
    "v_exp_f32 v67, v67\n"
    "v_exp_f32 v68, v68\n"
    "v_exp_f32 v69, v69\n"
    "v_mfma_f32_32x32x16_bf16 v[80:95], v[126:129], v[104:107], 0\n"
    "ds_read_b128 v[126:129], v150 offset:1024\n"
    "v_exp_f32 v70, v70\n"
    "v_exp_f32 v71, v71\n"
    "v_exp_f32 v72, v72\n"
    "v_exp_f32 v73, v73\n"
    "v_smfmac_f32_32x32x32_bf16 v[16:31], v[134:137], v[64:71], v112\n"
    "ds_read_b128 v[134:137], v151 offset:0\n"
    "v_exp_f32 v74, v74\n"
    "v_exp_f32 v75, v75\n"
    "v_exp_f32 v76, v76\n"
    "v_exp_f32 v77, v77\n"
    "v_mfma_f32_32x32x8_bf16 v[80:95], v[132:133], v[110:111], v[80:95]\n"
    "ds_read_b64 v[132:133], v115 offset:256\n"
    "v_exp_f32 v78, v78\n"
    "v_exp_f32 v79, v79\n"
    "v_exp_f32 v48, v48\n"
    "v_exp_f32 v49, v49\n"
    "v_smfmac_f32_32x32x32_bf16 v[16:31], v[138:141], v[72:79], v112\n"
    "ds_read_b128 v[138:141], v151 offset:640\n"
    "v_exp_f32 v50, v50\n"
    "v_exp_f32 v51, v51\n"
    "v_exp_f32 v52, v52\n"
    "v_exp_f32 v53, v53\n"
    "s_waitcnt lgkmcnt(4)\n"
    "v_mfma_f32_32x32x16_bf16 v[32:47], v[122:125], v[96:99], 0\n"
    "v_exp_f32 v54, v54\n"
    "v_exp_f32 v55, v55\n"
    "v_exp_f32 v56, v56\n"
    "v_exp_f32 v57, v57\n"
    "v_smfmac_f32_32x32x32_bf16 v[0:15], v[142:145], v[48:55], v112\n"
    "s_mov_b64 exec, 1\n"
    "ds_add_u32 v119, v112\n"
    "s_mov_b64 exec, -1\n"
    "v_exp_f32 v58, v58\n"
    "v_exp_f32 v59, v59\n"
    "v_exp_f32 v60, v60\n"
    "v_exp_f32 v61, v61\n"
    "v_mfma_f32_32x32x8_bf16 v[32:47], v[130:131], v[100:101], v[32:47]\n"
    "v_exp_f32 v62, v62\n"
    "v_exp_f32 v63, v63\n"
    "v_exp_f32 v80, v80\n"
    "v_exp_f32 v81, v81\n"
    "v_smfmac_f32_32x32x32_bf16 v[0:15], v[146:149], v[56:63], v112\n"
    "v_exp_f32 v82, v82\n"
    "v_exp_f32 v83, v83\n"
    "v_exp_f32 v84, v84\n"
    "v_exp_f32 v85, v85\n"
    "v_mfma_f32_32x32x16_bf16 v[64:79], v[122:125], v[104:107], 0\n"
    "ds_read_b128 v[122:125], v150 offset:2048\n"
    "v_exp_f32 v86, v86\n"
    "v_exp_f32 v87, v87\n"
    "v_exp_f32 v88, v88\n"
    "v_exp_f32 v89, v89\n"
    "v_smfmac_f32_32x32x32_bf16 v[16:31], v[142:145], v[80:87], v112\n"
    "ds_read_b128 v[142:145], v151 offset:1280\n"
    "v_exp_f32 v90, v90\n"
    "v_exp_f32 v91, v91\n"
    "v_exp_f32 v92, v92\n"
    "v_exp_f32 v93, v93\n"
    "v_mfma_f32_32x32x8_bf16 v[64:79], v[130:131], v[108:109], v[64:79]\n"
    "v_exp_f32 v94, v94\n"
    "v_exp_f32 v95, v95\n"
    "v_exp_f32 v32, v32\n"
    "v_exp_f32 v33, v33\n"
    "v_smfmac_f32_32x32x32_bf16 v[16:31], v[146:149], v[88:95], v112\n"
    "ds_read_b128 v[146:149], v151 offset:1920\n"
    "v_exp_f32 v34, v34\n"
    "v_exp_f32 v35, v35\n"
    "v_exp_f32 v36, v36\n"
    "v_exp_f32 v37, v37\n"
    "s_waitcnt lgkmcnt(4)\n"
    "v_mfma_f32_32x32x16_bf16 v[48:63], v[126:129], v[96:99], 0\n"
    "v_exp_f32 v38, v38\n"
    "v_exp_f32 v39, v39\n"
    "v_exp_f32 v40, v40\n"
    "v_exp_f32 v41, v41\n"
    "v_smfmac_f32_32x32x32_bf16 v[0:15], v[134:137], v[32:39], v112\n"
    "v_exp_f32 v42, v42\n"
    "v_exp_f32 v43, v43\n"
    "v_exp_f32 v44, v44\n"
    "v_exp_f32 v45, v45\n"
    "v_mfma_f32_32x32x8_bf16 v[48:63], v[130:131], v[102:103], v[48:63]\n"
    "v_exp_f32 v46, v46\n"
    "v_exp_f32 v47, v47\n"
    "v_exp_f32 v64, v64\n"
    "v_exp_f32 v65, v65\n"
    "v_smfmac_f32_32x32x32_bf16 v[0:15], v[138:141], v[40:47], v112\n"
    "v_exp_f32 v66, v66\n"
    "v_exp_f32 v67, v67\n"
    "v_exp_f32 v68, v68\n"
    "v_exp_f32 v69, v69\n"
    "v_mfma_f32_32x32x16_bf16 v[80:95], v[126:129], v[104:107], 0\n"
    "ds_read_b128 v[126:129], v150 offset:3072\n"
    "v_exp_f32 v70, v70\n"
    "v_exp_f32 v71, v71\n"
    "v_exp_f32 v72, v72\n"
    "v_exp_f32 v73, v73\n"
    "v_smfmac_f32_32x32x32_bf16 v[16:31], v[134:137], v[64:71], v112\n"
    "ds_read_b128 v[134:137], v151 offset:2560\n"
    "v_exp_f32 v74, v74\n"
    "v_exp_f32 v75, v75\n"
    "v_exp_f32 v76, v76\n"
    "v_exp_f32 v77, v77\n"
    "v_mfma_f32_32x32x8_bf16 v[80:95], v[130:131], v[110:111], v[80:95]\n"
    "ds_read_b64 v[130:131], v115 offset:512\n"
    "v_exp_f32 v78, v78\n"
    "v_exp_f32 v79, v79\n"
    "v_exp_f32 v48, v48\n"
    "v_exp_f32 v49, v49\n"
    "v_smfmac_f32_32x32x32_bf16 v[16:31], v[138:141], v[72:79], v112\n"
    "ds_read_b128 v[138:141], v151 offset:3200\n"
    "v_exp_f32 v50, v50\n"
    "v_exp_f32 v51, v51\n"
    "v_exp_f32 v52, v52\n"
    "v_exp_f32 v53, v53\n"
    "s_waitcnt lgkmcnt(4)\n"
    "v_mfma_f32_32x32x16_bf16 v[32:47], v[122:125], v[96:99], 0\n"
    "v_exp_f32 v54, v54\n"
    "v_exp_f32 v55, v55\n"
    "v_exp_f32 v56, v56\n"
    "v_exp_f32 v57, v57\n"
    "v_smfmac_f32_32x32x32_bf16 v[0:15], v[142:145], v[48:55], v112\n"
    "v_exp_f32 v58, v58\n"
    "v_exp_f32 v59, v59\n"
    "v_exp_f32 v60, v60\n"
    "v_exp_f32 v61, v61\n"
    "v_mfma_f32_32x32x8_bf16 v[32:47], v[132:133], v[100:101], v[32:47]\n"
    "v_exp_f32 v62, v62\n"
    "v_exp_f32 v63, v63\n"
    "v_exp_f32 v80, v80\n"
    "v_exp_f32 v81, v81\n"
    "v_smfmac_f32_32x32x32_bf16 v[0:15], v[146:149], v[56:63], v112\n"
    "v_exp_f32 v82, v82\n"
    "v_exp_f32 v83, v83\n"
    "v_exp_f32 v84, v84\n"
    "v_exp_f32 v85, v85\n"
    "v_mfma_f32_32x32x16_bf16 v[64:79], v[122:125], v[104:107], 0\n"
    "ds_read_b128 v[122:125], v150 offset:4096\n"
    "v_exp_f32 v86, v86\n"
    "v_exp_f32 v87, v87\n"
    "v_exp_f32 v88, v88\n"
    "v_exp_f32 v89, v89\n"
    "v_smfmac_f32_32x32x32_bf16 v[16:31], v[142:145], v[80:87], v112\n"
    "ds_read_b128 v[142:145], v151 offset:3840\n"
    "v_exp_f32 v90, v90\n"
    "v_exp_f32 v91, v91\n"
    "v_exp_f32 v92, v92\n"
    "v_exp_f32 v93, v93\n"
    "v_mfma_f32_32x32x8_bf16 v[64:79], v[132:133], v[108:109], v[64:79]\n"
    "v_exp_f32 v94, v94\n"
    "v_exp_f32 v95, v95\n"
    "v_exp_f32 v32, v32\n"
    "v_exp_f32 v33, v33\n"
    "v_smfmac_f32_32x32x32_bf16 v[16:31], v[146:149], v[88:95], v112\n"
    "ds_read_b128 v[146:149], v151 offset:4480\n"
    "v_exp_f32 v34, v34\n"
    "v_exp_f32 v35, v35\n"
    "v_exp_f32 v36, v36\n"
    "v_exp_f32 v37, v37\n"
    "s_waitcnt lgkmcnt(3)\n"
    "v_mfma_f32_32x32x16_bf16 v[48:63], v[126:129], v[96:99], 0\n"
    "v_exp_f32 v38, v38\n"
    "v_exp_f32 v39, v39\n"
    "v_exp_f32 v40, v40\n"
    "v_exp_f32 v41, v41\n"
    "v_smfmac_f32_32x32x32_bf16 v[0:15], v[134:137], v[32:39], v112\n"
    "v_exp_f32 v42, v42\n"
    "v_exp_f32 v43, v43\n"
    "v_exp_f32 v44, v44\n"
    "v_exp_f32 v45, v45\n"
    "v_mfma_f32_32x32x8_bf16 v[48:63], v[132:133], v[102:103], v[48:63]\n"
    "v_exp_f32 v46, v46\n"
    "v_exp_f32 v47, v47\n"
    "v_exp_f32 v64, v64\n"
    "v_exp_f32 v65, v65\n"
    "v_smfmac_f32_32x32x32_bf16 v[0:15], v[138:141], v[40:47], v112\n"
    "v_exp_f32 v66, v66\n"
    "v_exp_f32 v67, v67\n"
    "v_exp_f32 v68, v68\n"
    "v_exp_f32 v69, v69\n"
    "v_mfma_f32_32x32x16_bf16 v[80:95], v[126:129], v[104:107], 0\n"
    "ds_read_b128 v[126:129], v150 offset:5120\n"
    "v_exp_f32 v70, v70\n"
    "v_exp_f32 v71, v71\n"
    "v_exp_f32 v72, v72\n"
    "v_exp_f32 v73, v73\n"
    "v_smfmac_f32_32x32x32_bf16 v[16:31], v[134:137], v[64:71], v112\n"
    "ds_read_b128 v[134:137], v151 offset:5120\n"
    "v_exp_f32 v74, v74\n"
    "v_exp_f32 v75, v75\n"
    "v_exp_f32 v76, v76\n"
    "v_exp_f32 v77, v77\n"
    "v_mfma_f32_32x32x8_bf16 v[80:95], v[132:133], v[110:111], v[80:95]\n"
    "ds_read_b64 v[132:133], v115 offset:768\n"
    "v_exp_f32 v78, v78\n"
    "v_exp_f32 v79, v79\n"
    "v_exp_f32 v48, v48\n"
    "v_exp_f32 v49, v49\n"
    "v_smfmac_f32_32x32x32_bf16 v[16:31], v[138:141], v[72:79], v112\n"
    "ds_read_b128 v[138:141], v151 offset:5760\n"
    "v_exp_f32 v50, v50\n"
    "v_exp_f32 v51, v51\n"
    "v_exp_f32 v52, v52\n"
    "v_exp_f32 v53, v53\n"
    "s_waitcnt lgkmcnt(4)\n"
    "v_mfma_f32_32x32x16_bf16 v[32:47], v[122:125], v[96:99], 0\n"
    "v_exp_f32 v54, v54\n"
    "v_exp_f32 v55, v55\n"
    "v_exp_f32 v56, v56\n"
    "v_exp_f32 v57, v57\n"
    "v_smfmac_f32_32x32x32_bf16 v[0:15], v[142:145], v[48:55], v112\n"
    "v_exp_f32 v58, v58\n"
    "v_exp_f32 v59, v59\n"
    "v_exp_f32 v60, v60\n"
    "v_exp_f32 v61, v61\n"
    "v_mfma_f32_32x32x8_bf16 v[32:47], v[130:131], v[100:101], v[32:47]\n"
    "v_exp_f32 v62, v62\n"
    "v_exp_f32 v63, v63\n"
    "v_exp_f32 v80, v80\n"
    "v_exp_f32 v81, v81\n"
    "v_smfmac_f32_32x32x32_bf16 v[0:15], v[146:149], v[56:63], v112\n"
    "v_exp_f32 v82, v82\n"
    "v_exp_f32 v83, v83\n"
    "v_exp_f32 v84, v84\n"
    "v_exp_f32 v85, v85\n"
    "v_mfma_f32_32x32x16_bf16 v[64:79], v[122:125], v[104:107], 0\n"
    "ds_read_b128 v[122:125], v150 offset:6144\n"
    "v_exp_f32 v86, v86\n"
    "v_exp_f32 v87, v87\n"
    "v_exp_f32 v88, v88\n"
    "v_exp_f32 v89, v89\n"
    "v_smfmac_f32_32x32x32_bf16 v[16:31], v[142:145], v[80:87], v112\n"
    "ds_read_b128 v[142:145], v151 offset:6400\n"
    "v_exp_f32 v90, v90\n"
    "v_exp_f32 v91, v91\n"
    "v_exp_f32 v92, v92\n"
    "v_exp_f32 v93, v93\n"
    "v_mfma_f32_32x32x8_bf16 v[64:79], v[130:131], v[108:109], v[64:79]\n"
    "v_exp_f32 v94, v94\n"
    "v_exp_f32 v95, v95\n"
    "v_exp_f32 v32, v32\n"
    "v_exp_f32 v33, v33\n"
    "v_smfmac_f32_32x32x32_bf16 v[16:31], v[146:149], v[88:95], v112\n"
    "ds_read_b128 v[146:149], v151 offset:7040\n"
    "v_exp_f32 v34, v34\n"
    "v_exp_f32 v35, v35\n"
    "v_exp_f32 v36, v36\n"
    "v_exp_f32 v37, v37\n"
    "s_waitcnt lgkmcnt(3)\n"
    "v_mfma_f32_32x32x16_bf16 v[48:63], v[126:129], v[96:99], 0\n"
    "v_exp_f32 v38, v38\n"
    "v_exp_f32 v39, v39\n"
    "v_exp_f32 v40, v40\n"
    "v_exp_f32 v41, v41\n"
    "v_smfmac_f32_32x32x32_bf16 v[0:15], v[134:137], v[32:39], v112\n"
    "v_exp_f32 v42, v42\n"
    "v_exp_f32 v43, v43\n"
    "v_exp_f32 v44, v44\n"
    "v_exp_f32 v45, v45\n"
    "v_mfma_f32_32x32x8_bf16 v[48:63], v[130:131], v[102:103], v[48:63]\n"
    "v_exp_f32 v46, v46\n"
    "v_exp_f32 v47, v47\n"
    "v_exp_f32 v64, v64\n"
    "v_exp_f32 v65, v65\n"
    "v_smfmac_f32_32x32x32_bf16 v[0:15], v[138:141], v[40:47], v112\n"
    "v_exp_f32 v66, v66\n"
    "v_exp_f32 v67, v67\n"
    "v_exp_f32 v68, v68\n"
    "v_exp_f32 v69, v69\n"
    "v_mfma_f32_32x32x16_bf16 v[80:95], v[126:129], v[104:107], 0\n"
    "ds_read_b128 v[126:129], v150 offset:7168\n"
    "v_exp_f32 v70, v70\n"
    "v_exp_f32 v71, v71\n"
    "v_exp_f32 v72, v72\n"
    "v_exp_f32 v73, v73\n"
    "v_smfmac_f32_32x32x32_bf16 v[16:31], v[134:137], v[64:71], v112\n"
    "ds_read_b128 v[134:137], v151 offset:7680\n"
    "v_exp_f32 v74, v74\n"
    "v_exp_f32 v75, v75\n"
    "v_exp_f32 v76, v76\n"
    "v_exp_f32 v77, v77\n"
    "v_mfma_f32_32x32x8_bf16 v[80:95], v[130:131], v[110:111], v[80:95]\n"
    "v_exp_f32 v78, v78\n"
    "v_exp_f32 v79, v79\n"
    "ds_read_b32 v118, v117 offset:4\n"
    "v_exp_f32 v48, v48\n"
    "v_exp_f32 v49, v49\n"
    "v_smfmac_f32_32x32x32_bf16 v[16:31], v[138:141], v[72:79], v112\n"
    "ds_read_b128 v[138:141], v151 offset:8320\n"
    "v_exp_f32 v50, v50\n"
    "v_exp_f32 v51, v51\n"
    "v_exp_f32 v52, v52\n"
    "v_exp_f32 v53, v53\n"
    "s_waitcnt lgkmcnt(4)\n"
    "v_mfma_f32_32x32x16_bf16 v[32:47], v[122:125], v[96:99], 0\n"
    "v_exp_f32 v54, v54\n"
    "v_exp_f32 v55, v55\n"
    "v_exp_f32 v56, v56\n"
    "v_exp_f32 v57, v57\n"
    "v_smfmac_f32_32x32x32_bf16 v[0:15], v[142:145], v[48:55], v112\n"
    "v_exp_f32 v58, v58\n"
    "v_exp_f32 v59, v59\n"
    "v_exp_f32 v60, v60\n"
    "v_exp_f32 v61, v61\n"
    "v_mfma_f32_32x32x8_bf16 v[32:47], v[132:133], v[100:101], v[32:47]\n"
    "v_exp_f32 v62, v62\n"
    "v_exp_f32 v63, v63\n"
    "v_exp_f32 v80, v80\n"
    "v_exp_f32 v81, v81\n"
    "v_smfmac_f32_32x32x32_bf16 v[0:15], v[146:149], v[56:63], v112\n"
    "v_exp_f32 v82, v82\n"
    "v_exp_f32 v83, v83\n"
    "v_exp_f32 v84, v84\n"
    "v_exp_f32 v85, v85\n"
    "v_mfma_f32_32x32x16_bf16 v[64:79], v[122:125], v[104:107], 0\n"
    "v_exp_f32 v86, v86\n"
    "v_exp_f32 v87, v87\n"
    "v_exp_f32 v88, v88\n"
    "v_exp_f32 v89, v89\n"
    "v_smfmac_f32_32x32x32_bf16 v[16:31], v[142:145], v[80:87], v112\n"
    "ds_read_b128 v[142:145], v151 offset:8960\n"
    "v_exp_f32 v90, v90\n"
    "v_exp_f32 v91, v91\n"
    "v_exp_f32 v92, v92\n"
    "v_exp_f32 v93, v93\n"
    "v_mfma_f32_32x32x8_bf16 v[64:79], v[132:133], v[108:109], v[64:79]\n"
    "v_exp_f32 v94, v94\n"
    "v_exp_f32 v95, v95\n"
    "v_exp_f32 v32, v32\n"
    "v_exp_f32 v33, v33\n"
    "v_smfmac_f32_32x32x32_bf16 v[16:31], v[146:149], v[88:95], v112\n"
    "ds_read_b128 v[146:149], v151 offset:9600\n"
    "v_exp_f32 v34, v34\n"
    "v_exp_f32 v35, v35\n"
    "v_exp_f32 v36, v36\n"
    "v_exp_f32 v37, v37\n"
    "s_waitcnt lgkmcnt(2)\n"
    "v_mfma_f32_32x32x16_bf16 v[48:63], v[126:129], v[96:99], 0\n"
    "v_readfirstlane_b32 s41, v118\n"
    "v_exp_f32 v38, v38\n"
    "v_exp_f32 v39, v39\n"
    "v_exp_f32 v40, v40\n"
    "v_exp_f32 v41, v41\n"
    "v_smfmac_f32_32x32x32_bf16 v[0:15], v[134:137], v[32:39], v112\n"
    "s_cmp_ge_u32 s41, 4\n"
    "s_cbranch_scc0 31f\n"
    "21:\n"
    "v_exp_f32 v42, v42\n"
    "ds_read_b128 v[122:125], v150 offset:19456\n"
    "ds_read_b64 v[130:131], v121 offset:0\n"
    "v_exp_f32 v43, v43\n"
    "v_exp_f32 v44, v44\n"
    "v_exp_f32 v45, v45\n"
    "v_mfma_f32_32x32x8_bf16 v[48:63], v[132:133], v[102:103], v[48:63]\n"
    "v_exp_f32 v46, v46\n"
    "v_exp_f32 v47, v47\n"
    "v_exp_f32 v64, v64\n"
    "v_exp_f32 v65, v65\n"
    "v_smfmac_f32_32x32x32_bf16 v[0:15], v[138:141], v[40:47], v112\n"
    "v_exp_f32 v66, v66\n"
    "v_exp_f32 v67, v67\n"
    "v_exp_f32 v68, v68\n"
    "v_exp_f32 v69, v69\n"
    "v_mfma_f32_32x32x16_bf16 v[80:95], v[126:129], v[104:107], 0\n"
    "ds_read_b128 v[126:129], v150 offset:20480\n"
    "v_exp_f32 v70, v70\n"
    "v_exp_f32 v71, v71\n"
    "v_exp_f32 v72, v72\n"
    "v_exp_f32 v73, v73\n"
    "v_smfmac_f32_32x32x32_bf16 v[16:31], v[134:137], v[64:71], v112\n"
    "ds_read_b128 v[134:137], v151 offset:19456\n"
    "v_exp_f32 v74, v74\n"
    "v_exp_f32 v75, v75\n"
    "v_exp_f32 v76, v76\n"
    "v_exp_f32 v77, v77\n"
    "v_mfma_f32_32x32x8_bf16 v[80:95], v[132:133], v[110:111], v[80:95]\n"
    "ds_read_b64 v[132:133], v121 offset:256\n"
    "v_exp_f32 v78, v78\n"
    "v_exp_f32 v79, v79\n"
    "v_exp_f32 v48, v48\n"
    "v_exp_f32 v49, v49\n"
    "v_smfmac_f32_32x32x32_bf16 v[16:31], v[138:141], v[72:79], v112\n"
    "ds_read_b128 v[138:141], v151 offset:20096\n"
    "v_exp_f32 v50, v50\n"
    "v_exp_f32 v51, v51\n"
    "v_exp_f32 v52, v52\n"
    "v_exp_f32 v53, v53\n"
    "s_waitcnt lgkmcnt(4)\n"
    "v_mfma_f32_32x32x16_bf16 v[32:47], v[122:125], v[96:99], 0\n"
    "v_exp_f32 v54, v54\n"
    "v_exp_f32 v55, v55\n"
    "v_exp_f32 v56, v56\n"
    "v_exp_f32 v57, v57\n"
    "v_smfmac_f32_32x32x32_bf16 v[0:15], v[142:145], v[48:55], v112\n"
    "s_mov_b64 exec, 1\n"
    "ds_add_u32 v119, v112\n"
    "s_mov_b64 exec, -1\n"
    "v_exp_f32 v58, v58\n"
    "v_exp_f32 v59, v59\n"
    "v_exp_f32 v60, v60\n"
    "v_exp_f32 v61, v61\n"
    "v_mfma_f32_32x32x8_bf16 v[32:47], v[130:131], v[100:101], v[32:47]\n"
    "v_exp_f32 v62, v62\n"
    "v_exp_f32 v63, v63\n"
    "v_exp_f32 v80, v80\n"
    "v_exp_f32 v81, v81\n"
    "v_smfmac_f32_32x32x32_bf16 v[0:15], v[146:149], v[56:63], v112\n"
    "v_exp_f32 v82, v82\n"
    "v_exp_f32 v83, v83\n"
    "v_exp_f32 v84, v84\n"
    "v_exp_f32 v85, v85\n"
    "v_mfma_f32_32x32x16_bf16 v[64:79], v[122:125], v[104:107], 0\n"
    "ds_read_b128 v[122:125], v150 offset:21504\n"
    "v_exp_f32 v86, v86\n"
    "v_exp_f32 v87, v87\n"
    "v_exp_f32 v88, v88\n"
    "v_exp_f32 v89, v89\n"
    "v_smfmac_f32_32x32x32_bf16 v[16:31], v[142:145], v[80:87], v112\n"
    "ds_read_b128 v[142:145], v151 offset:20736\n"
    "v_exp_f32 v90, v90\n"
    "v_exp_f32 v91, v91\n"
    "v_exp_f32 v92, v92\n"
    "v_exp_f32 v93, v93\n"
    "v_mfma_f32_32x32x8_bf16 v[64:79], v[130:131], v[108:109], v[64:79]\n"
    "v_exp_f32 v94, v94\n"
    "v_exp_f32 v95, v95\n"
    "v_exp_f32 v32, v32\n"
    "v_exp_f32 v33, v33\n"
    "v_smfmac_f32_32x32x32_bf16 v[16:31], v[146:149], v[88:95], v112\n"
    "ds_read_b128 v[146:149], v151 offset:21376\n"
    "v_exp_f32 v34, v34\n"
    "v_exp_f32 v35, v35\n"
    "v_exp_f32 v36, v36\n"
    "v_exp_f32 v37, v37\n"
    "s_waitcnt lgkmcnt(4)\n"
    "v_mfma_f32_32x32x16_bf16 v[48:63], v[126:129], v[96:99], 0\n"
    "v_exp_f32 v38, v38\n"
    "v_exp_f32 v39, v39\n"
    "v_exp_f32 v40, v40\n"
    "v_exp_f32 v41, v41\n"
    "v_smfmac_f32_32x32x32_bf16 v[0:15], v[134:137], v[32:39], v112\n"
    "v_exp_f32 v42, v42\n"
    "v_exp_f32 v43, v43\n"
    "v_exp_f32 v44, v44\n"
    "v_exp_f32 v45, v45\n"
    "v_mfma_f32_32x32x8_bf16 v[48:63], v[130:131], v[102:103], v[48:63]\n"
    "v_exp_f32 v46, v46\n"
    "v_exp_f32 v47, v47\n"
    "v_exp_f32 v64, v64\n"
    "v_exp_f32 v65, v65\n"
    "v_smfmac_f32_32x32x32_bf16 v[0:15], v[138:141], v[40:47], v112\n"
    "v_exp_f32 v66, v66\n"
    "v_exp_f32 v67, v67\n"
    "v_exp_f32 v68, v68\n"
    "v_exp_f32 v69, v69\n"
    "v_mfma_f32_32x32x16_bf16 v[80:95], v[126:129], v[104:107], 0\n"
    "ds_read_b128 v[126:129], v150 offset:22528\n"
    "v_exp_f32 v70, v70\n"
    "v_exp_f32 v71, v71\n"
    "v_exp_f32 v72, v72\n"
    "v_exp_f32 v73, v73\n"
    "v_smfmac_f32_32x32x32_bf16 v[16:31], v[134:137], v[64:71], v112\n"
    "ds_read_b128 v[134:137], v151 offset:22016\n"
    "v_exp_f32 v74, v74\n"
    "v_exp_f32 v75, v75\n"
    "v_exp_f32 v76, v76\n"
    "v_exp_f32 v77, v77\n"
    "v_mfma_f32_32x32x8_bf16 v[80:95], v[130:131], v[110:111], v[80:95]\n"
    "ds_read_b64 v[130:131], v121 offset:512\n"
    "v_exp_f32 v78, v78\n"
    "v_exp_f32 v79, v79\n"
    "v_exp_f32 v48, v48\n"
    "v_exp_f32 v49, v49\n"
    "v_smfmac_f32_32x32x32_bf16 v[16:31], v[138:141], v[72:79], v112\n"
    "ds_read_b128 v[138:141], v151 offset:22656\n"
    "v_exp_f32 v50, v50\n"
    "v_exp_f32 v51, v51\n"
    "v_exp_f32 v52, v52\n"
    "v_exp_f32 v53, v53\n"
    "s_waitcnt lgkmcnt(4)\n"
    "v_mfma_f32_32x32x16_bf16 v[32:47], v[122:125], v[96:99], 0\n"
    "v_exp_f32 v54, v54\n"
    "v_exp_f32 v55, v55\n"
    "v_exp_f32 v56, v56\n"
    "v_exp_f32 v57, v57\n"
    "v_smfmac_f32_32x32x32_bf16 v[0:15], v[142:145], v[48:55], v112\n"
    "v_exp_f32 v58, v58\n"
    "v_exp_f32 v59, v59\n"
    "v_exp_f32 v60, v60\n"
    "v_exp_f32 v61, v61\n"
    "v_mfma_f32_32x32x8_bf16 v[32:47], v[132:133], v[100:101], v[32:47]\n"
    "v_exp_f32 v62, v62\n"
    "v_exp_f32 v63, v63\n"
    "v_exp_f32 v80, v80\n"
    "v_exp_f32 v81, v81\n"
    "v_smfmac_f32_32x32x32_bf16 v[0:15], v[146:149], v[56:63], v112\n"
    "v_exp_f32 v82, v82\n"
    "v_exp_f32 v83, v83\n"
    "v_exp_f32 v84, v84\n"
    "v_exp_f32 v85, v85\n"
    "v_mfma_f32_32x32x16_bf16 v[64:79], v[122:125], v[104:107], 0\n"
    "ds_read_b128 v[122:125], v150 offset:23552\n"
    "v_exp_f32 v86, v86\n"
    "v_exp_f32 v87, v87\n"
    "v_exp_f32 v88, v88\n"
    "v_exp_f32 v89, v89\n"
    "v_smfmac_f32_32x32x32_bf16 v[16:31], v[142:145], v[80:87], v112\n"
    "ds_read_b128 v[142:145], v151 offset:23296\n"
    "v_exp_f32 v90, v90\n"
    "v_exp_f32 v91, v91\n"
    "v_exp_f32 v92, v92\n"
    "v_exp_f32 v93, v93\n"
    "v_mfma_f32_32x32x8_bf16 v[64:79], v[132:133], v[108:109], v[64:79]\n"
    "v_exp_f32 v94, v94\n"
    "v_exp_f32 v95, v95\n"
    "v_exp_f32 v32, v32\n"
    "v_exp_f32 v33, v33\n"
    "v_smfmac_f32_32x32x32_bf16 v[16:31], v[146:149], v[88:95], v112\n"
    "ds_read_b128 v[146:149], v151 offset:23936\n"
    "v_exp_f32 v34, v34\n"
    "v_exp_f32 v35, v35\n"
    "v_exp_f32 v36, v36\n"
    "v_exp_f32 v37, v37\n"
    "s_waitcnt lgkmcnt(3)\n"
    "v_mfma_f32_32x32x16_bf16 v[48:63], v[126:129], v[96:99], 0\n"
    "v_exp_f32 v38, v38\n"
    "v_exp_f32 v39, v39\n"
    "v_exp_f32 v40, v40\n"
    "v_exp_f32 v41, v41\n"
    "v_smfmac_f32_32x32x32_bf16 v[0:15], v[134:137], v[32:39], v112\n"
    "v_exp_f32 v42, v42\n"
    "v_exp_f32 v43, v43\n"
    "v_exp_f32 v44, v44\n"
    "v_exp_f32 v45, v45\n"
    "v_mfma_f32_32x32x8_bf16 v[48:63], v[132:133], v[102:103], v[48:63]\n"
    "v_exp_f32 v46, v46\n"
    "v_exp_f32 v47, v47\n"
    "v_exp_f32 v64, v64\n"
    "v_exp_f32 v65, v65\n"
    "v_smfmac_f32_32x32x32_bf16 v[0:15], v[138:141], v[40:47], v112\n"
    "v_exp_f32 v66, v66\n"
    "v_exp_f32 v67, v67\n"
    "v_exp_f32 v68, v68\n"
    "v_exp_f32 v69, v69\n"
    "v_mfma_f32_32x32x16_bf16 v[80:95], v[126:129], v[104:107], 0\n"
    "ds_read_b128 v[126:129], v150 offset:24576\n"
    "v_exp_f32 v70, v70\n"
    "v_exp_f32 v71, v71\n"
    "v_exp_f32 v72, v72\n"
    "v_exp_f32 v73, v73\n"
    "v_smfmac_f32_32x32x32_bf16 v[16:31], v[134:137], v[64:71], v112\n"
    "ds_read_b128 v[134:137], v151 offset:24576\n"
    "v_exp_f32 v74, v74\n"
    "v_exp_f32 v75, v75\n"
    "v_exp_f32 v76, v76\n"
    "v_exp_f32 v77, v77\n"
    "v_mfma_f32_32x32x8_bf16 v[80:95], v[132:133], v[110:111], v[80:95]\n"
    "ds_read_b64 v[132:133], v121 offset:768\n"
    "v_exp_f32 v78, v78\n"
    "v_exp_f32 v79, v79\n"
    "v_exp_f32 v48, v48\n"
    "v_exp_f32 v49, v49\n"
    "v_smfmac_f32_32x32x32_bf16 v[16:31], v[138:141], v[72:79], v112\n"
    "ds_read_b128 v[138:141], v151 offset:25216\n"
    "v_exp_f32 v50, v50\n"
    "v_exp_f32 v51, v51\n"
    "v_exp_f32 v52, v52\n"
    "v_exp_f32 v53, v53\n"
    "s_waitcnt lgkmcnt(4)\n"
    "v_mfma_f32_32x32x16_bf16 v[32:47], v[122:125], v[96:99], 0\n"
    "v_exp_f32 v54, v54\n"
    "v_exp_f32 v55, v55\n"
    "v_exp_f32 v56, v56\n"
    "v_exp_f32 v57, v57\n"
    "v_smfmac_f32_32x32x32_bf16 v[0:15], v[142:145], v[48:55], v112\n"
    "v_exp_f32 v58, v58\n"
    "v_exp_f32 v59, v59\n"
    "v_exp_f32 v60, v60\n"
    "v_exp_f32 v61, v61\n"
    "v_mfma_f32_32x32x8_bf16 v[32:47], v[130:131], v[100:101], v[32:47]\n"
    "v_exp_f32 v62, v62\n"
    "v_exp_f32 v63, v63\n"
    "v_exp_f32 v80, v80\n"
    "v_exp_f32 v81, v81\n"
    "v_smfmac_f32_32x32x32_bf16 v[0:15], v[146:149], v[56:63], v112\n"
    "v_exp_f32 v82, v82\n"
    "v_exp_f32 v83, v83\n"
    "v_exp_f32 v84, v84\n"
    "v_exp_f32 v85, v85\n"
    "v_mfma_f32_32x32x16_bf16 v[64:79], v[122:125], v[104:107], 0\n"
    "ds_read_b128 v[122:125], v150 offset:25600\n"
    "v_exp_f32 v86, v86\n"
    "v_exp_f32 v87, v87\n"
    "v_exp_f32 v88, v88\n"
    "v_exp_f32 v89, v89\n"
    "v_smfmac_f32_32x32x32_bf16 v[16:31], v[142:145], v[80:87], v112\n"
    "ds_read_b128 v[142:145], v151 offset:25856\n"
    "v_exp_f32 v90, v90\n"
    "v_exp_f32 v91, v91\n"
    "v_exp_f32 v92, v92\n"
    "v_exp_f32 v93, v93\n"
    "v_mfma_f32_32x32x8_bf16 v[64:79], v[130:131], v[108:109], v[64:79]\n"
    "v_exp_f32 v94, v94\n"
    "v_exp_f32 v95, v95\n"
    "v_exp_f32 v32, v32\n"
    "v_exp_f32 v33, v33\n"
    "v_smfmac_f32_32x32x32_bf16 v[16:31], v[146:149], v[88:95], v112\n"
    "ds_read_b128 v[146:149], v151 offset:26496\n"
    "v_exp_f32 v34, v34\n"
    "v_exp_f32 v35, v35\n"
    "v_exp_f32 v36, v36\n"
    "v_exp_f32 v37, v37\n"
    "s_waitcnt lgkmcnt(3)\n"
    "v_mfma_f32_32x32x16_bf16 v[48:63], v[126:129], v[96:99], 0\n"
    "v_exp_f32 v38, v38\n"
    "v_exp_f32 v39, v39\n"
    "v_exp_f32 v40, v40\n"
    "v_exp_f32 v41, v41\n"
    "v_smfmac_f32_32x32x32_bf16 v[0:15], v[134:137], v[32:39], v112\n"
    "v_exp_f32 v42, v42\n"
    "v_exp_f32 v43, v43\n"
    "v_exp_f32 v44, v44\n"
    "v_exp_f32 v45, v45\n"
    "v_mfma_f32_32x32x8_bf16 v[48:63], v[130:131], v[102:103], v[48:63]\n"
    "v_exp_f32 v46, v46\n"
    "v_exp_f32 v47, v47\n"
    "v_exp_f32 v64, v64\n"
    "v_exp_f32 v65, v65\n"
    "v_smfmac_f32_32x32x32_bf16 v[0:15], v[138:141], v[40:47], v112\n"
    "v_exp_f32 v66, v66\n"
    "v_exp_f32 v67, v67\n"
    "v_exp_f32 v68, v68\n"
    "v_exp_f32 v69, v69\n"
    "v_mfma_f32_32x32x16_bf16 v[80:95], v[126:129], v[104:107], 0\n"
    "ds_read_b128 v[126:129], v150 offset:26624\n"
    "v_exp_f32 v70, v70\n"
    "v_exp_f32 v71, v71\n"
    "v_exp_f32 v72, v72\n"
    "v_exp_f32 v73, v73\n"
    "v_smfmac_f32_32x32x32_bf16 v[16:31], v[134:137], v[64:71], v112\n"
    "ds_read_b128 v[134:137], v151 offset:27136\n"
    "v_exp_f32 v74, v74\n"
    "v_exp_f32 v75, v75\n"
    "v_exp_f32 v76, v76\n"
    "v_exp_f32 v77, v77\n"
    "v_mfma_f32_32x32x8_bf16 v[80:95], v[130:131], v[110:111], v[80:95]\n"
    "v_exp_f32 v78, v78\n"
    "v_exp_f32 v79, v79\n"
    "ds_read_b32 v118, v117 offset:8\n"
    "v_exp_f32 v48, v48\n"
    "v_exp_f32 v49, v49\n"
    "v_smfmac_f32_32x32x32_bf16 v[16:31], v[138:141], v[72:79], v112\n"
    "ds_read_b128 v[138:141], v151 offset:27776\n"
    "v_exp_f32 v50, v50\n"
    "v_exp_f32 v51, v51\n"
    "v_exp_f32 v52, v52\n"
    "v_exp_f32 v53, v53\n"
    "s_waitcnt lgkmcnt(4)\n"
    "v_mfma_f32_32x32x16_bf16 v[32:47], v[122:125], v[96:99], 0\n"
    "v_exp_f32 v54, v54\n"
    "v_exp_f32 v55, v55\n"
    "v_exp_f32 v56, v56\n"
    "v_exp_f32 v57, v57\n"
    "v_smfmac_f32_32x32x32_bf16 v[0:15], v[142:145], v[48:55], v112\n"
    "v_exp_f32 v58, v58\n"
    "v_exp_f32 v59, v59\n"
    "v_exp_f32 v60, v60\n"
    "v_exp_f32 v61, v61\n"
    "v_mfma_f32_32x32x8_bf16 v[32:47], v[132:133], v[100:101], v[32:47]\n"
    "v_exp_f32 v62, v62\n"
    "v_exp_f32 v63, v63\n"
    "v_exp_f32 v80, v80\n"
    "v_exp_f32 v81, v81\n"
    "v_smfmac_f32_32x32x32_bf16 v[0:15], v[146:149], v[56:63], v112\n"
    "v_exp_f32 v82, v82\n"
    "v_exp_f32 v83, v83\n"
    "v_exp_f32 v84, v84\n"
    "v_exp_f32 v85, v85\n"
    "v_mfma_f32_32x32x16_bf16 v[64:79], v[122:125], v[104:107], 0\n"
    "v_exp_f32 v86, v86\n"
    "v_exp_f32 v87, v87\n"
    "v_exp_f32 v88, v88\n"
    "v_exp_f32 v89, v89\n"
    "v_smfmac_f32_32x32x32_bf16 v[16:31], v[142:145], v[80:87], v112\n"
    "ds_read_b128 v[142:145], v151 offset:28416\n"
    "v_exp_f32 v90, v90\n"
    "v_exp_f32 v91, v91\n"
    "v_exp_f32 v92, v92\n"
    "v_exp_f32 v93, v93\n"
    "v_mfma_f32_32x32x8_bf16 v[64:79], v[132:133], v[108:109], v[64:79]\n"
    "v_exp_f32 v94, v94\n"
    "v_exp_f32 v95, v95\n"
    "v_exp_f32 v32, v32\n"
    "v_exp_f32 v33, v33\n"
    "v_smfmac_f32_32x32x32_bf16 v[16:31], v[146:149], v[88:95], v112\n"
    "ds_read_b128 v[146:149], v151 offset:29056\n"
    "v_exp_f32 v34, v34\n"
    "v_exp_f32 v35, v35\n"
    "v_exp_f32 v36, v36\n"
    "v_exp_f32 v37, v37\n"
    "s_waitcnt lgkmcnt(2)\n"
    "v_mfma_f32_32x32x16_bf16 v[48:63], v[126:129], v[96:99], 0\n"
    "v_readfirstlane_b32 s41, v118\n"
    "v_exp_f32 v38, v38\n"
    "v_exp_f32 v39, v39\n"
    "v_exp_f32 v40, v40\n"
    "v_exp_f32 v41, v41\n"
    "v_smfmac_f32_32x32x32_bf16 v[0:15], v[134:137], v[32:39], v112\n"
    "s_cmp_ge_u32 s41, 4\n"
    "s_cbranch_scc0 32f\n"
    "22:\n"
    "v_exp_f32 v42, v42\n"
    "ds_read_b128 v[122:125], v113 offset:0\n"
    "ds_read_b64 v[130:131], v114 offset:0\n"
    "v_exp_f32 v43, v43\n"
    "v_exp_f32 v44, v44\n"
    "v_exp_f32 v45, v45\n"
    "v_mfma_f32_32x32x8_bf16 v[48:63], v[132:133], v[102:103], v[48:63]\n"
    "v_exp_f32 v46, v46\n"
    "v_exp_f32 v47, v47\n"
    "v_exp_f32 v64, v64\n"
    "v_exp_f32 v65, v65\n"
    "v_smfmac_f32_32x32x32_bf16 v[0:15], v[138:141], v[40:47], v112\n"
    "v_exp_f32 v66, v66\n"
    "v_exp_f32 v67, v67\n"
    "v_exp_f32 v68, v68\n"
    "v_exp_f32 v69, v69\n"
    "v_mfma_f32_32x32x16_bf16 v[80:95], v[126:129], v[104:107], 0\n"
    "ds_read_b128 v[126:129], v113 offset:1024\n"
    "v_exp_f32 v70, v70\n"
    "v_exp_f32 v71, v71\n"
    "v_exp_f32 v72, v72\n"
    "v_exp_f32 v73, v73\n"
    "v_smfmac_f32_32x32x32_bf16 v[16:31], v[134:137], v[64:71], v112\n"
    "ds_read_b128 v[134:137], v116 offset:0\n"
    "v_exp_f32 v74, v74\n"
    "v_exp_f32 v75, v75\n"
    "v_exp_f32 v76, v76\n"
    "v_exp_f32 v77, v77\n"
    "v_mfma_f32_32x32x8_bf16 v[80:95], v[132:133], v[110:111], v[80:95]\n"
    "ds_read_b64 v[132:133], v114 offset:256\n"
    "v_exp_f32 v78, v78\n"
    "v_exp_f32 v79, v79\n"
    "v_exp_f32 v48, v48\n"
    "v_exp_f32 v49, v49\n"
    "v_smfmac_f32_32x32x32_bf16 v[16:31], v[138:141], v[72:79], v112\n"
    "ds_read_b128 v[138:141], v116 offset:640\n"
    "v_exp_f32 v50, v50\n"
    "v_exp_f32 v51, v51\n"
    "v_exp_f32 v52, v52\n"
    "v_exp_f32 v53, v53\n"
    "s_waitcnt lgkmcnt(4)\n"
    "v_mfma_f32_32x32x16_bf16 v[32:47], v[122:125], v[96:99], 0\n"
    "v_exp_f32 v54, v54\n"
    "v_exp_f32 v55, v55\n"
    "v_exp_f32 v56, v56\n"
    "v_exp_f32 v57, v57\n"
    "v_smfmac_f32_32x32x32_bf16 v[0:15], v[142:145], v[48:55], v112\n"
    "s_mov_b64 exec, 1\n"
    "ds_add_u32 v119, v112\n"
    "s_mov_b64 exec, -1\n"
    "v_exp_f32 v58, v58\n"
    "v_exp_f32 v59, v59\n"
    "v_exp_f32 v60, v60\n"
    "v_exp_f32 v61, v61\n"
    "v_mfma_f32_32x32x8_bf16 v[32:47], v[130:131], v[100:101], v[32:47]\n"
    "v_exp_f32 v62, v62\n"
    "v_exp_f32 v63, v63\n"
    "v_exp_f32 v80, v80\n"
    "v_exp_f32 v81, v81\n"
    "v_smfmac_f32_32x32x32_bf16 v[0:15], v[146:149], v[56:63], v112\n"
    "v_exp_f32 v82, v82\n"
    "v_exp_f32 v83, v83\n"
    "v_exp_f32 v84, v84\n"
    "v_exp_f32 v85, v85\n"
    "v_mfma_f32_32x32x16_bf16 v[64:79], v[122:125], v[104:107], 0\n"
    "ds_read_b128 v[122:125], v113 offset:2048\n"
    "v_exp_f32 v86, v86\n"
    "v_exp_f32 v87, v87\n"
    "v_exp_f32 v88, v88\n"
    "v_exp_f32 v89, v89\n"
    "v_smfmac_f32_32x32x32_bf16 v[16:31], v[142:145], v[80:87], v112\n"
    "ds_read_b128 v[142:145], v116 offset:1280\n"
    "v_exp_f32 v90, v90\n"
    "v_exp_f32 v91, v91\n"
    "v_exp_f32 v92, v92\n"
    "v_exp_f32 v93, v93\n"
    "v_mfma_f32_32x32x8_bf16 v[64:79], v[130:131], v[108:109], v[64:79]\n"
    "v_exp_f32 v94, v94\n"
    "v_exp_f32 v95, v95\n"
    "v_exp_f32 v32, v32\n"
    "v_exp_f32 v33, v33\n"
    "v_smfmac_f32_32x32x32_bf16 v[16:31], v[146:149], v[88:95], v112\n"
    "ds_read_b128 v[146:149], v116 offset:1920\n"
    "v_exp_f32 v34, v34\n"
    "v_exp_f32 v35, v35\n"
    "v_exp_f32 v36, v36\n"
    "v_exp_f32 v37, v37\n"
    "s_waitcnt lgkmcnt(4)\n"
    "v_mfma_f32_32x32x16_bf16 v[48:63], v[126:129], v[96:99], 0\n"
    "v_exp_f32 v38, v38\n"
    "v_exp_f32 v39, v39\n"
    "v_exp_f32 v40, v40\n"
    "v_exp_f32 v41, v41\n"
    "v_smfmac_f32_32x32x32_bf16 v[0:15], v[134:137], v[32:39], v112\n"
    "v_exp_f32 v42, v42\n"
    "v_exp_f32 v43, v43\n"
    "v_exp_f32 v44, v44\n"
    "v_exp_f32 v45, v45\n"
    "v_mfma_f32_32x32x8_bf16 v[48:63], v[130:131], v[102:103], v[48:63]\n"
    "v_exp_f32 v46, v46\n"
    "v_exp_f32 v47, v47\n"
    "v_exp_f32 v64, v64\n"
    "v_exp_f32 v65, v65\n"
    "v_smfmac_f32_32x32x32_bf16 v[0:15], v[138:141], v[40:47], v112\n"
    "v_exp_f32 v66, v66\n"
    "v_exp_f32 v67, v67\n"
    "v_exp_f32 v68, v68\n"
    "v_exp_f32 v69, v69\n"
    "v_mfma_f32_32x32x16_bf16 v[80:95], v[126:129], v[104:107], 0\n"
    "ds_read_b128 v[126:129], v113 offset:3072\n"
    "v_exp_f32 v70, v70\n"
    "v_exp_f32 v71, v71\n"
    "v_exp_f32 v72, v72\n"
    "v_exp_f32 v73, v73\n"
    "v_smfmac_f32_32x32x32_bf16 v[16:31], v[134:137], v[64:71], v112\n"
    "ds_read_b128 v[134:137], v116 offset:2560\n"
    "v_exp_f32 v74, v74\n"
    "v_exp_f32 v75, v75\n"
    "v_exp_f32 v76, v76\n"
    "v_exp_f32 v77, v77\n"
    "v_mfma_f32_32x32x8_bf16 v[80:95], v[130:131], v[110:111], v[80:95]\n"
    "ds_read_b64 v[130:131], v114 offset:512\n"
    "v_exp_f32 v78, v78\n"
    "v_exp_f32 v79, v79\n"
    "v_exp_f32 v48, v48\n"
    "v_exp_f32 v49, v49\n"
    "v_smfmac_f32_32x32x32_bf16 v[16:31], v[138:141], v[72:79], v112\n"
    "ds_read_b128 v[138:141], v116 offset:3200\n"
    "v_exp_f32 v50, v50\n"
    "v_exp_f32 v51, v51\n"
    "v_exp_f32 v52, v52\n"
    "v_exp_f32 v53, v53\n"
    "s_waitcnt lgkmcnt(4)\n"
    "v_mfma_f32_32x32x16_bf16 v[32:47], v[122:125], v[96:99], 0\n"
    "v_exp_f32 v54, v54\n"
    "v_exp_f32 v55, v55\n"
    "v_exp_f32 v56, v56\n"
    "v_exp_f32 v57, v57\n"
    "v_smfmac_f32_32x32x32_bf16 v[0:15], v[142:145], v[48:55], v112\n"
    "v_exp_f32 v58, v58\n"
    "v_exp_f32 v59, v59\n"
    "v_exp_f32 v60, v60\n"
    "v_exp_f32 v61, v61\n"
    "v_mfma_f32_32x32x8_bf16 v[32:47], v[132:133], v[100:101], v[32:47]\n"
    "v_exp_f32 v62, v62\n"
    "v_exp_f32 v63, v63\n"
    "v_exp_f32 v80, v80\n"
    "v_exp_f32 v81, v81\n"
    "v_smfmac_f32_32x32x32_bf16 v[0:15], v[146:149], v[56:63], v112\n"
    "v_exp_f32 v82, v82\n"
    "v_exp_f32 v83, v83\n"
    "v_exp_f32 v84, v84\n"
    "v_exp_f32 v85, v85\n"
    "v_mfma_f32_32x32x16_bf16 v[64:79], v[122:125], v[104:107], 0\n"
    "ds_read_b128 v[122:125], v113 offset:4096\n"
    "v_exp_f32 v86, v86\n"
    "v_exp_f32 v87, v87\n"
    "v_exp_f32 v88, v88\n"
    "v_exp_f32 v89, v89\n"
    "v_smfmac_f32_32x32x32_bf16 v[16:31], v[142:145], v[80:87], v112\n"
    "ds_read_b128 v[142:145], v116 offset:3840\n"
    "v_exp_f32 v90, v90\n"
    "v_exp_f32 v91, v91\n"
    "v_exp_f32 v92, v92\n"
    "v_exp_f32 v93, v93\n"
    "v_mfma_f32_32x32x8_bf16 v[64:79], v[132:133], v[108:109], v[64:79]\n"
    "v_exp_f32 v94, v94\n"
    "v_exp_f32 v95, v95\n"
    "v_exp_f32 v32, v32\n"
    "v_exp_f32 v33, v33\n"
    "v_smfmac_f32_32x32x32_bf16 v[16:31], v[146:149], v[88:95], v112\n"
    "ds_read_b128 v[146:149], v116 offset:4480\n"
    "v_exp_f32 v34, v34\n"
    "v_exp_f32 v35, v35\n"
    "v_exp_f32 v36, v36\n"
    "v_exp_f32 v37, v37\n"
    "s_waitcnt lgkmcnt(3)\n"
    "v_mfma_f32_32x32x16_bf16 v[48:63], v[126:129], v[96:99], 0\n"
    "v_exp_f32 v38, v38\n"
    "v_exp_f32 v39, v39\n"
    "v_exp_f32 v40, v40\n"
    "v_exp_f32 v41, v41\n"
    "v_smfmac_f32_32x32x32_bf16 v[0:15], v[134:137], v[32:39], v112\n"
    "v_exp_f32 v42, v42\n"
    "v_exp_f32 v43, v43\n"
    "v_exp_f32 v44, v44\n"
    "v_exp_f32 v45, v45\n"
    "v_mfma_f32_32x32x8_bf16 v[48:63], v[132:133], v[102:103], v[48:63]\n"
    "v_exp_f32 v46, v46\n"
    "v_exp_f32 v47, v47\n"
    "v_exp_f32 v64, v64\n"
    "v_exp_f32 v65, v65\n"
    "v_smfmac_f32_32x32x32_bf16 v[0:15], v[138:141], v[40:47], v112\n"
    "v_exp_f32 v66, v66\n"
    "v_exp_f32 v67, v67\n"
    "v_exp_f32 v68, v68\n"
    "v_exp_f32 v69, v69\n"
    "v_mfma_f32_32x32x16_bf16 v[80:95], v[126:129], v[104:107], 0\n"
    "ds_read_b128 v[126:129], v113 offset:5120\n"
    "v_exp_f32 v70, v70\n"
    "v_exp_f32 v71, v71\n"
    "v_exp_f32 v72, v72\n"
    "v_exp_f32 v73, v73\n"
    "v_smfmac_f32_32x32x32_bf16 v[16:31], v[134:137], v[64:71], v112\n"
    "ds_read_b128 v[134:137], v116 offset:5120\n"
    "v_exp_f32 v74, v74\n"
    "v_exp_f32 v75, v75\n"
    "v_exp_f32 v76, v76\n"
    "v_exp_f32 v77, v77\n"
    "v_mfma_f32_32x32x8_bf16 v[80:95], v[132:133], v[110:111], v[80:95]\n"
    "ds_read_b64 v[132:133], v114 offset:768\n"
    "v_exp_f32 v78, v78\n"
    "v_exp_f32 v79, v79\n"
    "v_exp_f32 v48, v48\n"
    "v_exp_f32 v49, v49\n"
    "v_smfmac_f32_32x32x32_bf16 v[16:31], v[138:141], v[72:79], v112\n"
    "ds_read_b128 v[138:141], v116 offset:5760\n"
    "v_exp_f32 v50, v50\n"
    "v_exp_f32 v51, v51\n"
    "v_exp_f32 v52, v52\n"
    "v_exp_f32 v53, v53\n"
    "s_waitcnt lgkmcnt(4)\n"
    "v_mfma_f32_32x32x16_bf16 v[32:47], v[122:125], v[96:99], 0\n"
    "v_exp_f32 v54, v54\n"
    "v_exp_f32 v55, v55\n"
    "v_exp_f32 v56, v56\n"
    "v_exp_f32 v57, v57\n"
    "v_smfmac_f32_32x32x32_bf16 v[0:15], v[142:145], v[48:55], v112\n"
    "v_exp_f32 v58, v58\n"
    "v_exp_f32 v59, v59\n"
    "v_exp_f32 v60, v60\n"
    "v_exp_f32 v61, v61\n"
    "v_mfma_f32_32x32x8_bf16 v[32:47], v[130:131], v[100:101], v[32:47]\n"
    "v_exp_f32 v62, v62\n"
    "v_exp_f32 v63, v63\n"
    "v_exp_f32 v80, v80\n"
    "v_exp_f32 v81, v81\n"
    "v_smfmac_f32_32x32x32_bf16 v[0:15], v[146:149], v[56:63], v112\n"
    "v_exp_f32 v82, v82\n"
    "v_exp_f32 v83, v83\n"
    "v_exp_f32 v84, v84\n"
    "v_exp_f32 v85, v85\n"
    "v_mfma_f32_32x32x16_bf16 v[64:79], v[122:125], v[104:107], 0\n"
    "ds_read_b128 v[122:125], v113 offset:6144\n"
    "v_exp_f32 v86, v86\n"
    "v_exp_f32 v87, v87\n"
    "v_exp_f32 v88, v88\n"
    "v_exp_f32 v89, v89\n"
    "v_smfmac_f32_32x32x32_bf16 v[16:31], v[142:145], v[80:87], v112\n"
    "ds_read_b128 v[142:145], v116 offset:6400\n"
    "v_exp_f32 v90, v90\n"
    "v_exp_f32 v91, v91\n"
    "v_exp_f32 v92, v92\n"
    "v_exp_f32 v93, v93\n"
    "v_mfma_f32_32x32x8_bf16 v[64:79], v[130:131], v[108:109], v[64:79]\n"
    "v_exp_f32 v94, v94\n"
    "v_exp_f32 v95, v95\n"
    "v_exp_f32 v32, v32\n"
    "v_exp_f32 v33, v33\n"
    "v_smfmac_f32_32x32x32_bf16 v[16:31], v[146:149], v[88:95], v112\n"
    "ds_read_b128 v[146:149], v116 offset:7040\n"
    "v_exp_f32 v34, v34\n"
    "v_exp_f32 v35, v35\n"
    "v_exp_f32 v36, v36\n"
    "v_exp_f32 v37, v37\n"
    "s_waitcnt lgkmcnt(3)\n"
    "v_mfma_f32_32x32x16_bf16 v[48:63], v[126:129], v[96:99], 0\n"
    "v_exp_f32 v38, v38\n"
    "v_exp_f32 v39, v39\n"
    "v_exp_f32 v40, v40\n"
    "v_exp_f32 v41, v41\n"
    "v_smfmac_f32_32x32x32_bf16 v[0:15], v[134:137], v[32:39], v112\n"
    "v_exp_f32 v42, v42\n"
    "v_exp_f32 v43, v43\n"
    "v_exp_f32 v44, v44\n"
    "v_exp_f32 v45, v45\n"
    "v_mfma_f32_32x32x8_bf16 v[48:63], v[130:131], v[102:103], v[48:63]\n"
    "v_exp_f32 v46, v46\n"
    "v_exp_f32 v47, v47\n"
    "v_exp_f32 v64, v64\n"
    "v_exp_f32 v65, v65\n"
    "v_smfmac_f32_32x32x32_bf16 v[0:15], v[138:141], v[40:47], v112\n"
    "v_exp_f32 v66, v66\n"
    "v_exp_f32 v67, v67\n"
    "v_exp_f32 v68, v68\n"
    "v_exp_f32 v69, v69\n"
    "v_mfma_f32_32x32x16_bf16 v[80:95], v[126:129], v[104:107], 0\n"
    "ds_read_b128 v[126:129], v113 offset:7168\n"
    "v_exp_f32 v70, v70\n"
    "v_exp_f32 v71, v71\n"
    "v_exp_f32 v72, v72\n"
    "v_exp_f32 v73, v73\n"
    "v_smfmac_f32_32x32x32_bf16 v[16:31], v[134:137], v[64:71], v112\n"
    "ds_read_b128 v[134:137], v116 offset:7680\n"
    "v_exp_f32 v74, v74\n"
    "v_exp_f32 v75, v75\n"
    "v_exp_f32 v76, v76\n"
    "v_exp_f32 v77, v77\n"
    "v_mfma_f32_32x32x8_bf16 v[80:95], v[130:131], v[110:111], v[80:95]\n"
    "v_exp_f32 v78, v78\n"
    "v_exp_f32 v79, v79\n"
    "v_exp_f32 v48, v48\n"
    "v_exp_f32 v49, v49\n"
    "v_smfmac_f32_32x32x32_bf16 v[16:31], v[138:141], v[72:79], v112\n"
    "ds_read_b128 v[138:141], v116 offset:8320\n"
    "v_exp_f32 v50, v50\n"
    "v_exp_f32 v51, v51\n"
    "v_exp_f32 v52, v52\n"
    "v_exp_f32 v53, v53\n"
    "s_waitcnt lgkmcnt(3)\n"
    "v_mfma_f32_32x32x16_bf16 v[32:47], v[122:125], v[96:99], 0\n"
    "v_exp_f32 v54, v54\n"
    "v_exp_f32 v55, v55\n"
    "v_exp_f32 v56, v56\n"
    "v_exp_f32 v57, v57\n"
    "v_smfmac_f32_32x32x32_bf16 v[0:15], v[142:145], v[48:55], v112\n"
    "v_exp_f32 v58, v58\n"
    "v_exp_f32 v59, v59\n"
    "v_exp_f32 v60, v60\n"
    "v_exp_f32 v61, v61\n"
    "v_mfma_f32_32x32x8_bf16 v[32:47], v[132:133], v[100:101], v[32:47]\n"
    "v_exp_f32 v62, v62\n"
    "v_exp_f32 v63, v63\n"
    "v_exp_f32 v80, v80\n"
    "v_exp_f32 v81, v81\n"
    "v_smfmac_f32_32x32x32_bf16 v[0:15], v[146:149], v[56:63], v112\n"
    "v_exp_f32 v82, v82\n"
    "v_exp_f32 v83, v83\n"
    "v_exp_f32 v84, v84\n"
    "v_exp_f32 v85, v85\n"
    "v_mfma_f32_32x32x16_bf16 v[64:79], v[122:125], v[104:107], 0\n"
    "v_exp_f32 v86, v86\n"
    "v_exp_f32 v87, v87\n"
    "v_exp_f32 v88, v88\n"
    "v_exp_f32 v89, v89\n"
    "v_smfmac_f32_32x32x32_bf16 v[16:31], v[142:145], v[80:87], v112\n"
    "ds_read_b128 v[142:145], v116 offset:8960\n"
    "v_exp_f32 v90, v90\n"
    "v_exp_f32 v91, v91\n"
    "v_exp_f32 v92, v92\n"
    "v_exp_f32 v93, v93\n"
    "v_mfma_f32_32x32x8_bf16 v[64:79], v[132:133], v[108:109], v[64:79]\n"
    "v_exp_f32 v94, v94\n"
    "v_exp_f32 v95, v95\n"
    "v_exp_f32 v32, v32\n"
    "v_exp_f32 v33, v33\n"
    "v_smfmac_f32_32x32x32_bf16 v[16:31], v[146:149], v[88:95], v112\n"
    "ds_read_b128 v[146:149], v116 offset:9600\n"
    "v_exp_f32 v34, v34\n"
    "v_exp_f32 v35, v35\n"
    "v_exp_f32 v36, v36\n"
    "v_exp_f32 v37, v37\n"
    "s_waitcnt lgkmcnt(2)\n"
    "v_mfma_f32_32x32x16_bf16 v[48:63], v[126:129], v[96:99], 0\n"
    "v_exp_f32 v38, v38\n"
    "v_exp_f32 v39, v39\n"
    "v_exp_f32 v40, v40\n"
    "v_exp_f32 v41, v41\n"
    "v_smfmac_f32_32x32x32_bf16 v[0:15], v[134:137], v[32:39], v112\n"
    "v_exp_f32 v42, v42\n"
    "v_exp_f32 v43, v43\n"
    "v_exp_f32 v44, v44\n"
    "v_exp_f32 v45, v45\n"
    "v_mfma_f32_32x32x8_bf16 v[48:63], v[132:133], v[102:103], v[48:63]\n"
    "v_exp_f32 v46, v46\n"
    "v_exp_f32 v47, v47\n"
    "v_exp_f32 v64, v64\n"
    "v_exp_f32 v65, v65\n"
    "v_smfmac_f32_32x32x32_bf16 v[0:15], v[138:141], v[40:47], v112\n"
    "v_exp_f32 v66, v66\n"
    "v_exp_f32 v67, v67\n"
    "v_exp_f32 v68, v68\n"
    "v_exp_f32 v69, v69\n"
    "v_mfma_f32_32x32x16_bf16 v[80:95], v[126:129], v[104:107], 0\n"
    "v_exp_f32 v70, v70\n"
    "v_exp_f32 v71, v71\n"
    "v_exp_f32 v72, v72\n"
    "v_exp_f32 v73, v73\n"
    "v_smfmac_f32_32x32x32_bf16 v[16:31], v[134:137], v[64:71], v112\n"
    "v_exp_f32 v74, v74\n"
    "v_exp_f32 v75, v75\n"
    "v_exp_f32 v76, v76\n"
    "v_exp_f32 v77, v77\n"
    "v_mfma_f32_32x32x8_bf16 v[80:95], v[132:133], v[110:111], v[80:95]\n"
    "v_exp_f32 v78, v78\n"
    "v_exp_f32 v79, v79\n"
    "v_exp_f32 v48, v48\n"
    "v_exp_f32 v49, v49\n"
    "v_smfmac_f32_32x32x32_bf16 v[16:31], v[138:141], v[72:79], v112\n"
    "v_exp_f32 v50, v50\n"
    "v_exp_f32 v51, v51\n"
    "v_exp_f32 v52, v52\n"
    "v_exp_f32 v53, v53\n"
    "s_waitcnt lgkmcnt(0)\n"
    "v_exp_f32 v54, v54\n"
    "v_exp_f32 v55, v55\n"
    "v_exp_f32 v56, v56\n"
    "v_exp_f32 v57, v57\n"
    "v_smfmac_f32_32x32x32_bf16 v[0:15], v[142:145], v[48:55], v112\n"
    "s_mov_b64 exec, 1\n"
    "ds_add_u32 v119, v112\n"
    "s_mov_b64 exec, -1\n"
    "v_exp_f32 v58, v58\n"
    "v_exp_f32 v59, v59\n"
    "v_exp_f32 v60, v60\n"
    "v_exp_f32 v61, v61\n"
    "v_exp_f32 v62, v62\n"
    "v_exp_f32 v63, v63\n"
    "v_exp_f32 v80, v80\n"
    "v_exp_f32 v81, v81\n"
    "v_smfmac_f32_32x32x32_bf16 v[0:15], v[146:149], v[56:63], v112\n"
    "v_exp_f32 v82, v82\n"
    "v_exp_f32 v83, v83\n"
    "v_exp_f32 v84, v84\n"
    "v_exp_f32 v85, v85\n"
    "v_exp_f32 v86, v86\n"
    "v_exp_f32 v87, v87\n"
    "v_exp_f32 v88, v88\n"
    "v_exp_f32 v89, v89\n"
    "v_smfmac_f32_32x32x32_bf16 v[16:31], v[142:145], v[80:87], v112\n"
    "v_exp_f32 v90, v90\n"
    "v_exp_f32 v91, v91\n"
    "v_exp_f32 v92, v92\n"
    "v_exp_f32 v93, v93\n"
    "v_exp_f32 v94, v94\n"
    "v_exp_f32 v95, v95\n"
    "s_nop 1\n"
    "v_smfmac_f32_32x32x32_bf16 v[16:31], v[146:149], v[88:95], v112\n"
    "s_waitcnt lgkmcnt(0)\n"
    "s_branch 5f\n"
    "30:\n"
    "s_mov_b32 s42, 0\n"
    "40:\n"
    "s_sleep 2\n"
    "ds_read_b32 v118, v117 offset:0\n"
    "s_add_u32 s42, s42, 1\n"
    "s_waitcnt lgkmcnt(0)\n"
    "v_readfirstlane_b32 s41, v118\n"
    "s_cmp_gt_u32 s42, 0x100000\n"
    "s_cbranch_scc1 20b\n"
    "s_nop 3\n"
    "s_cmp_ge_u32 s41, 4\n"
    "s_cbranch_scc0 40b\n"
    "s_branch 20b\n"
    "31:\n"
    "s_mov_b32 s42, 0\n"
    "41:\n"
    "s_sleep 2\n"
    "ds_read_b32 v118, v117 offset:4\n"
    "s_add_u32 s42, s42, 1\n"
    "s_waitcnt lgkmcnt(0)\n"
    "v_readfirstlane_b32 s41, v118\n"
    "s_cmp_gt_u32 s42, 0x100000\n"
    "s_cbranch_scc1 21b\n"
    "s_nop 3\n"
    "s_cmp_ge_u32 s41, 4\n"
    "s_cbranch_scc0 41b\n"
    "s_branch 21b\n"
    "32:\n"
    "s_mov_b32 s42, 0\n"
    "42:\n"
    "s_sleep 2\n"
    "ds_read_b32 v118, v117 offset:8\n"
    "s_add_u32 s42, s42, 1\n"
    "s_waitcnt lgkmcnt(0)\n"
    "v_readfirstlane_b32 s41, v118\n"
    "s_cmp_gt_u32 s42, 0x100000\n"
    "s_cbranch_scc1 22b\n"
    "s_nop 3\n"
    "s_cmp_ge_u32 s41, 4\n"
    "s_cbranch_scc0 42b\n"
    "s_branch 22b\n"
    "5:\n"
    "s_nop 15\n"
          : "+{v[0:15]}"(O0), "+{v[16:31]}"(O1)
          : "{v[96:99]}"(QM[0]), "{v[100:101]}"(QA[0]), "{v[102:103]}"(QB[0]), "{v[104:107]}"(QM[1]), "{v[108:109]}"(QA[1]),
            "{v[110:111]}"(QB[1]), "{v112}"(idx), "{v113}"(ka), "{v114}"(kra), "{v115}"(krn), "{v116}"(va), "{v117}"(flg),
            "{v119}"(prg), "{v121}"(krm), "{v150}"(kan), "{v151}"(van)
          : "memory", "scc", "s41", "s42", "v118",
            "v32", "v33", "v34", "v35", "v36", "v37", "v38", "v39", "v40", "v41", "v42", "v43", "v44", "v45", "v46", "v47",
            "v48", "v49", "v50", "v51", "v52", "v53", "v54", "v55", "v56", "v57", "v58", "v59", "v60", "v61", "v62", "v63",
            "v64", "v65", "v66", "v67", "v68", "v69", "v70", "v71", "v72", "v73", "v74", "v75", "v76", "v77", "v78", "v79",
            "v80", "v81", "v82", "v83", "v84", "v85", "v86", "v87", "v88", "v89", "v90", "v91", "v92", "v93", "v94", "v95",
            "v122", "v123", "v124", "v125", "v126", "v127", "v128", "v129", "v130", "v131", "v132", "v133", "v134", "v135", "v136", "v137",
            "v138", "v139", "v140", "v141", "v142", "v143", "v144", "v145", "v146", "v147", "v148", "v149"
      );
    }
    TS(6);
    {
      if (kh == 0) {
        __builtin_amdgcn_s_setprio(2);
        wait_words<2>(SY(PROG), 4, 4 * PU, lane);
#pragma unroll
        for (int c = 0; c < 2; ++c) {
          const f16v& O = c ? O1 : O0;
          lw128(exa0 + c * 2048, __builtin_bit_cast(u4v, f4v{O[0], O[1], O[2], O[3]}));
          lw128(exa0 + c * 2048 + 1024, __builtin_bit_cast(u4v, f4v{O[4], O[5], O[6], O[7]}));
          if (hf == 0) lw128(exb0 + c * 512, __builtin_bit_cast(u4v, f4v{O[8], O[9], O[10], mm[c]}));
        }
        sig(SY(OF + hl));
        unsigned wof[3];
#pragma unroll
        for (int n = 0; n < 3; ++n) { const int q = 64 * (n < 2 ? hl + 4 * n : 8) + lane; wof[n] = (unsigned)((q / 9) * 1152 + (q % 9) * 16); }
        wait_ge<16>(SY(HS3), 4); wait_ge<16>(SY(RDYC + 8), 4);
        TS(10);
        _Pragma("unroll 1") for (int blk = 0; blk < 9; ++blk) {
          const char* src = (const char*)wo + blk * 73728 + 144 * hg;
          const int j0 = 9 * blk + hl, j1 = j0 + 4, j2 = 9 * blk + 8;
          if (j0 != 75) dma(src, wof[0], j0 < 75 ? L_WO + j0 * 1024 : L_CRED + j0 * 1024 - 77760);
          else { if (lane < 60) dma(src, wof[0], L_WO + 75 * 1024); if (lane >= 60) dma(src, wof[0], L_CRED + 75 * 1024 - 77760); }
          dma(src, wof[1], j1 < 75 ? L_WO + j1 * 1024 : L_CRED + j1 * 1024 - 77760);
          if ((blk & 3) == hl) dma(src, wof[2], j2 < 75 ? L_WO + j2 * 1024 : L_CRED + j2 * 1024 - 77760);
        }
        VMW(0); sig(SY(RDYW));
        TS(11);
      } else {
        __builtin_amdgcn_s_setprio(3);
        u4v p0[2], p1[2], p2[2]; unsigned otag, rdy;
        _Pragma("unroll 1") for (unsigned sp = 0; sp < SPIN_CAP; ++sp) {
          asm volatile("ds_read_b32 %0, %8\n\tds_read_b32 %1, %9\n\tds_read_b128 %2, %10\n\tds_read_b128 %3, %10 offset:512\n\t"
                       "ds_read_b128 %4, %11\n\tds_read_b128 %5, %11 offset:1024\n\tds_read_b128 %6, %11 offset:2048\n\t"
                       "ds_read_b128 %7, %11 offset:3072\n\ts_waitcnt lgkmcnt(0)"
                       : "=&v"(rdy), "=&v"(otag), "=&v"(p2[0]), "=&v"(p2[1]), "=&v"(p0[0]), "=&v"(p1[0]), "=&v"(p0[1]), "=&v"(p1[1])
                       : "v"(SY(OF) + hl * 4), "v"(SY(TAGW)), "v"(exb0), "v"(exa0) : "memory");
          if (rfl(rdy)) break;
          __builtin_amdgcn_s_sleep(2);
        }
#pragma unroll
        for (int c = 0; c < 2; ++c) {
          const f16v& O = c ? O1 : O0;
          const f4v v0 = __builtin_bit_cast(f4v, p0[c]), v1 = __builtin_bit_cast(f4v, p1[c]), v2 = __builtin_bit_cast(f4v, p2[c]);
          const float M = fmaxf(mm[c], v2[3]), fo = __builtin_amdgcn_exp2f(mm[c] - M), fp = __builtin_amdgcn_exp2f(v2[3] - M);
          const float inv = __builtin_amdgcn_rcpf(__uint_as_float(sw32lo(__float_as_uint(O[10] * fo + v2[2] * fp))));
          const float go = fo * inv, gp = fp * inv;
          const f4v r0 = f4v{O[0], O[1], O[2], O[3]} * go + v0 * gp, r1 = f4v{O[4], O[5], O[6], O[7]} * go + v1 * gp;
          __builtin_amdgcn_raw_buffer_store_b128(u4v{pk(r0[0], r0[1]), pk(r0[2], r0[3]), pk(r1[0], r1[1]), pk(r1[2], r1[3])}, rs, (int)obm, c * 36864, 16);
          if (hf == 0) __builtin_amdgcn_raw_buffer_store_b32(pk(O[8] * go + v2[0] * gp, O[9] * go + v2[1] * gp), rs, (int)obr, c * 36864, 16);
        }
        TS(10);
        VMW(0);
        TS(11);
        if (lane < 8) gst32(ws, WS_FO + t * 1024 + lane * 128 + hg * 16 + hl * 4, otag);
        sig(SY(OPUB));
      }
      wait_ge<4>(SY(RDYW), 4);
    }
  } else {
    __builtin_amdgcn_s_setprio(2);
    const int h = w - 8, i = lane & 15, g = lane >> 4;
    unsigned tag;
    asm volatile("global_load_dword %0, %1, %2 sc1\n\ts_barrier\n\ts_waitcnt vmcnt(0)" : "=&v"(tag) : "v"(WS_E), "s"(ws) : "memory");
    tag += 1u;
    barrier();
    TS(15);
    if (h < 2) {
      const int row = 16 * kb + i;
      const unsigned xb = L_X + (row >> 3) * 1024 + (row & 7) * 128;
      const unsigned x0 = xb + ((g ^ ((row >> 1) & 7)) << 4), x1 = xb + (((4 + g) ^ ((row >> 1) & 7)) << 4);
      const unsigned wa = opq(L_WKV + (8 * g + ((lane >> 2) & 3)) * 48 + (h == 0 ? 8 * (lane & 3) : 40));
      u4v wfr[18], xf[18];
      f4v acc = {0, 0, 0, 0};
      auto ld = [&](int s0) {
#pragma unroll
        for (int s = s0; s < s0 + 3; ++s) {
          wfr[s] = ltr2(wa + s * 1536, wa + s * 1536 + 192);
          xf[s] = lr128(((s & 1) ? x1 : x0) + (s >> 1) * 8192);
        }
      };
      if (hg & 1) {
        ld(0);
#pragma unroll
        for (int s0 = 0; s0 < 18; s0 += 3) {
          if (s0 + 3 < 18) ld(s0 + 3);
          SB();
#pragma unroll
          for (int s = s0; s < s0 + 3; ++s) acc = mf16(xf[s], wfr[s], acc);
          SB();
        }
      } else {
        ld(0);
#pragma unroll
        for (int s0 = 0; s0 < 18; s0 += 3) {
          if (s0 + 3 < 18) ld(s0 + 3);
          SB();
#pragma unroll
          for (int s = s0; s < s0 + 3; ++s) acc = mf16(wfr[s], xf[s], acc);
          SB();
        }
      }
      sig(SY(KVDONE));
      TS(12);
      const int B = 4 * t + kb, T = B >> 1;
      const unsigned cbs = WS_KV + (B >> 4) * CHB;
      const unsigned p0 = pk(acc[0], acc[1]), p1 = pk(acc[2], acc[3]);
      float n2 = 0.f;
      if (!(hg & 1)) {
        if (h == 0) {
          const unsigned q0 = sw32hi(p0), q1 = sw32hi(p1);
          if (lane < 32) st128(rs, cbs + (T & 7) * 1024 + g * 512 + (16 * (B & 1) + i) * 16, u4v{p0, p1, q0, q1});
          n2 = blo(p0) * blo(p0) + bhi(p0) * bhi(p0) + blo(p1) * blo(p1) + bhi(p1) * bhi(p1);
          n2 = xsum32(xsum16(n2));
        } else {
          if (lane < 16) st32(rs, cbs + 8192 + ((T & 7) >> 1) * 256 + (16 * (B & 1) + i) * 8 + (T & 1) * 4, p1);
          n2 = blo(p1) * blo(p1) + bhi(p1) * bhi(p1);
        }
        n2 = rmax16(n2);
      } else {
        const unsigned q0 = sw16odd(p0), q1 = sw16odd(p1);
        u4v val = {p0, p1, q0, q1};
        int vr = i;
        if (h == 1) {
          vr = i == 2 ? 16 : i == 3 ? 17 : i == 0 ? 18 : 19;
          if (i == 0) val = u4v{0x3f803f80u, 0x3f803f80u, 0x3f803f80u, 0x3f803f80u};
          if (i == 1) val = u4v{0, 0, 0, 0};
        }
        if (!(g & 1) && (h == 0 || i < 4)) st128(rs, cbs + 9216 + (B & 15) * 640 + (g >> 1) * 320 + vr * 16, val);
      }
      if (lane == 0) lds_st(SY(KMAXP + h), __float_as_uint(n2));
      VMW(0);
      if (sig_rtn(SY(PUBA)) == 1) {
        const float km = __uint_as_float(lds_ld(SY(KMAXP))) + __uint_as_float(lds_ld(SY(KMAXP + 1)));
        if (lane < 8) gst64(ws, WS_FA + lane * 2048 + b * 8, ((u64)__float_as_uint(km) << 32) | tag);
      }
      TS(3);
    } else {
      const int pa = 2 * h - 4;
      const unsigned ba = wqoff(64 * pa + lane), bb = wqoff(64 * pa + 64 + lane);
      __builtin_amdgcn_s_setprio(0);
#pragma unroll
      for (int p = 2; p < 9; ++p) {
        if (p == 6) wait_ge(SY(KVDONE), 2);
        issue_b(ba, pa, p); issue_b(bb, pa + 1, p);
        if (p >= 5) { VMW(6); sig(SY(RP + p - 3)); }
      }
      TS(17);
      VMW(4); sig(SY(RP + 6));
      VMW(2); sig(SY(RP + 7));
      VMW(0); sig(SY(RP + 8));
      __builtin_amdgcn_s_setprio(2);
      TS(18);
    }
    const int chA = 72 * hg + 2 * (b & 31), chC = 72 * hg + 64 + ((b & 31) >> 2);
    u4v pz[3];
    {
      u4v hs0, hs1, hs2; unsigned da, dc;
      asm volatile("s_load_dwordx4 %0, %5, 0x0\n\ts_load_dwordx4 %1, %5, 0x1000\n\ts_load_dwordx4 %2, %6, 0x0\n\t"
                   "s_load_dword %3, %7, 0x0\n\ts_load_dword %4, %8, 0x0\n\ts_waitcnt lgkmcnt(0)"
                   : "=&s"(hs0), "=&s"(hs1), "=&s"(hs2), "=&s"(da), "=&s"(dc)
                   : "s"(ssk + chA * 2048), "s"(ssk + chC * 2048), "s"(ssd + chA), "s"(ssd + (chC & ~1)) : "memory");
#pragma unroll
      for (int X = 0; X < 3; ++X) {
        u4v h0 = X == 0 ? hs0 : X == 1 ? hs1 : hs2;
        const float dd = X == 0 ? blo(da) : X == 1 ? bhi(da) : (chC & 1) ? bhi(dc) : blo(dc);
        h0[0] = (h0[0] & 0xffff0000u) | (pk(blo(h0[0]) + dd, 0.f) & 0xffffu);
        h0 = h == 3 ? h0 : h == 2 ? u4v{0, h0[0], h0[1], h0[2]} : h == 1 ? u4v{0, 0, h0[0], h0[1]} : u4v{0, 0, 0, h0[0]};
        pz[X] = lane == 3 ? h0 : u4v{0, 0, 0, 0};
      }
    }
    const unsigned l16c = lane == 63 ? 992u : l16;
    const unsigned zoff = g * (576 * 128) + i * 8;
    const unsigned padd = lane < 4 ? h * HS_ + l16 : lane < 32 ? XO_ + (h & 1) * XS_ + 4096 + (l16 - 64) : XO_ + XS_ + 4544;
    unsigned ko[3];
#pragma unroll
    for (int n = 0; n < 2; ++n) {
      const int q = 64 * (h + 4 * n) + lane, pos = q / 9, row = 8 * (pos & 7) + (pos >> 3);
      ko[n] = (unsigned)(((64 * t + row) * 576 + 72 * hg + 8 * (q % 9)) * 2);
    }
    {
      const int q = 512 + lane, pos = q / 9, row = 8 * (pos & 7) + (pos >> 3), l2 = lane < 27 ? lane : 26;
      int sh = 64 * t - 3 + l2 / 9; sh = sh < 0 ? 0 : sh;
      ko[2] = h < 2 ? (unsigned)(((64 * t + row) * 576 + 72 * hg + 8 * (q % 9)) * 2) : (unsigned)((sh * 576 + 72 * hg + 8 * (l2 % 9)) * 2);
    }
    const unsigned wo0 = (unsigned)((72 * hg + 16 * h + 2 * (lane >> 3)) * 8), wo1 = (unsigned)((72 * hg + 64 + 2 * h) * 8);
    if (h == 0) {
      float km = 0.f;
      wait_ge<2>(SY(RP + PPOLL), 6);
      TS(16);
      u4v x0 = {0, 0, 0, 0}, x1 = {0, 0, 0, 0};
      bool need = true;
      _Pragma("unroll 1") for (unsigned s = 0; s < SPIN_CAP; ++s) {
        if (s == 1) TS(17);
        if (s == 2) TS(18);
        if (s == 3) TS(19);
        if (need) asm volatile("global_load_dwordx4 %0, %2, %3 sc1\n\tglobal_load_dwordx4 %1, %2, %3 offset:1024 sc1\n\ts_waitcnt vmcnt(0)"
                               : "+v"(x0), "+v"(x1) : "v"(WS_FA + (b & 7) * 2048 + lane * 16), "s"(ws) : "memory");
        need = !(x0[0] == tag && x0[2] == tag && x1[0] == tag && x1[2] == tag);
        if (!__any(need)) break;
        __builtin_amdgcn_s_sleep(2);
      }
      km = fmaxf(fmaxf(__uint_as_float(x0[1]), __uint_as_float(x0[3])), fmaxf(__uint_as_float(x1[1]), __uint_as_float(x1[3])));
      km = wmax(km);
      if (lane == 0) { lds_st(SY(TAGW), tag); lds_st(SY(KMAXV), __float_as_uint(km) | 1u); if (b == 0) gst32(ws, WS_E, tag); }
    }
    TS(4);
    wait_ge(SY(KMAXV), 1); wait_ge(SY(CQ4), 4);
    TS(5);
    chunk(0, 4); chunk(4, 5);
    VMW(5); sig(SY(RDYC + 0));
    TS(6);
    {
      KvJob j0; u4v w1;
      dma(gk, ko[0], kvt + h * 1024); dma(gv, ko[0], kvt + 9216 + h * 1024);
      dma(gk, ko[1], kvt + 4096 + h * 1024); dma(gv, ko[1], kvt + 13312 + h * 1024);
      dma(h == 1 ? gv : gk, ko[2], kvt + (h == 0 ? 8192 : h == 1 ? 17408 : 18432));
      asm volatile("global_load_dwordx4 %0, %2, %4\n\tglobal_load_dwordx4 %1, %3, %4\n\ts_waitcnt vmcnt(7)"
                   : "=&v"(j0.w), "=&v"(w1) : "v"(wo0), "v"(wo1), "s"(shk) : "memory");
      sig(SY(RDYC + 5));
      wait_ge(SY(QRDY), 4);
      chunk(1, 6); chunk(2, 7);
      asm volatile("s_waitcnt vmcnt(10)" : "+v"(j0.w), "+v"(w1) :: "memory");
      sig(SY(RDYKV));
      VMW(5); sig(SY(RDYC + 1));
      unsigned ea[4];
#pragma unroll
      for (int j = 0; j < 4; ++j) { const int p = lane - j; ea[j] = kvt + 128 + 4 * h + (p >= 0 ? ((p & 7) * 8 + (p >> 3)) * 144 : 18432 + (3 + p) * 144); }
      wait_ge(SY(RDYKV), 4);
      TS(13);
      kv_read(j0, kvt, h, lane);
      unsigned ek[4];
#pragma unroll
      for (int j = 0; j < 4; ++j) ek[j] = lr32(ea[j]);
      const unsigned ev = lr32(ea[0] + 9216);
      sig(SY(KVRD));
      u4v a0, a1;
      kv_calc(j0, t == 0 && (lane & 7) == 0, a0, a1);
      float e0 = fmul1(blo(ek[0]), blo(w1[0])), e1 = fmul1(bhi(ek[0]), blo(w1[2]));
#pragma unroll
      for (int j = 1; j < 4; ++j) {
        const unsigned kj = (t == 0 && lane < j) ? 0u : ek[j];
        const float c0 = (j & 1) ? bhi(w1[j >> 1]) : blo(w1[j >> 1]), c1 = (j & 1) ? bhi(w1[2 + (j >> 1)]) : blo(w1[2 + (j >> 1)]);
        e0 = ffma1(blo(kj), c0, e0); e1 = ffma1(bhi(kj), c1, e1);
      }
      const unsigned ep = pk(fmul1(e0, blo(ev)), fmul1(e1, bhi(ev)));
      const unsigned es = 8 * CHB + h * 256;
      *(LAS u16*)(size_t)(es + (63 - lane) * 2) = (u16)ep;
      *(LAS u16*)(size_t)(es + 128 + (63 - lane) * 2) = (u16)(ep >> 16);
      asm volatile("" ::: "memory");
      const u4v ex = lr128(es + i * 16);
      kv_store(rs, t, hg, h, lane, a0, a1);
      if (lane < 16) st128(rs, WS_XR + (unsigned)(72 * hg + 64 + 2 * h + (lane >> 3)) * XRS + 2 * (1984 - 64 * t) + (lane & 7) * 16, ex);
      if (t == 0 && lane < 2) st128(rs, WS_XR + (unsigned)(72 * hg + 64 + 2 * h + lane) * XRS + 4096, u4v{0, 0, 0, 0});
      VMW(3); sig(SY(RDYC + 2));
      TS(14);
    }
    wait_ge<2>(SY(DONEX), 8); wait_ge<2>(SY(KVRD), 4);
    TS(20);
#pragma unroll
    for (int X = 0; X < 3; ++X) {
      const int ch = X == 2 ? chC : chA + X;
      dma4((const char*)ssk + ch * 4096 + 4 + 4 * h, l16, l16c, L_CONV + X * CVS + h * HS_ + 64);
      lw128(L_CONV + X * CVS + padd, pz[X]);
    }
    VMW(12);
    if (sig_rtn(SY(PUBKV)) == 3 && lane < 32) gst32(ws, WS_FKV + (hg * 32 + lane) * 128 + (b & 31) * 4, tag);
    TS(22);
    const int sl = 2 * (i >> 2) + (i & 1) + 32 * ((i >> 1) & 1);
    const unsigned go0 = (unsigned)((64 * t + 8 * h + sl) * 576 + 72 * hg + 8 * g), go2 = (unsigned)((64 * t + 8 * g + sl) * 576 + 72 * hg + 64);
    gqv[0] = *(const u4v*)(gq + go0); gqv[1] = *(const u4v*)(gq + go0 + 32); gqv[2] = *(const u4v*)(gq + go2);
    goo[0] = go0 + (64 * t + 8 * h + sl) * 576; goo[1] = go2 + (64 * t + 8 * g + sl) * 576;
    asm volatile("" : "+v"(gqv[0]), "+v"(gqv[1]), "+v"(gqv[2]));
    int rf = h ? 0 : 8;
    const unsigned pa0 = SY(PROG) + (lane & 3) * 4;
    auto refill = [&](int k) {
      const int c = k == 0 ? 3 : k + 4, slot = k == 0 ? 4 : k == 1 ? 6 : k == 2 ? 7 : 5, p0 = h == 1 ? 0 : h == 2 ? 6 : 12;
      const char* src = ws + WS_KV + c * CHB + p0 * 1024;
      dma4_sc1(src, l16, slot * CHB + p0 * 1024);
      asm volatile("s_mov_b32 m0, %2\n\ts_nop 0\n\tglobal_load_lds_dwordx4 %0, %1 sc1\n\tglobal_load_lds_dwordx4 %0, %1 offset:1024 sc1\n\t"
                   "global_load_lds_dwordx4 %0, %1 offset:2048 sc1" :: "v"(l16), "s"(src + 4096), "s"(slot * CHB + p0 * 1024 + 4096) : "memory");
    };
    auto rword = [&](int k) -> unsigned { return SY(RDYC + (k == 0 ? 3 : k + 5)); };
    auto service = [&]() {
      if (rf & 1) { VMW(0); sig(rword(rf >> 1)); if (h == 1) sig(rword(rf >> 1)); ++rf; }
      if (rf < 8 && __all(lds_ld(pa0 + (rf == 6 ? 16 : 0)) >= (rf == 6 ? 1u : (unsigned)(rf >> 1) + 1) * PU)) { refill(rf >> 1); ++rf; }
    };
    if (h == 0) {
      const unsigned fo = WS_FKV + b * 128 + (lane & 31) * 4;
      __builtin_amdgcn_s_sleep(FKV_DELAY);
      poll<FKVP>(ws, fo, tag, [] {});
      if (lane == 0) lds_st(SY(FKVOK), 1u);
    } else {
      VMW(0);
      _Pragma("unroll 1") for (unsigned s = 0; s < SPIN_CAP; ++s) { if (rfl(lds_ld(SY(FKVOK)))) break; service(); __builtin_amdgcn_s_sleep(4); }
    }
    TS(23);
#pragma unroll
    for (int n = 0; n < 6; ++n) {
      const int X = n >> 1, p = n & 1;
      const int ch = X == 2 ? chC : chA + X;
      dma_sc1(ws + WS_XR + ch * XRS + (1 - p) * 2 + h * 1024, l16, L_CONV + X * CVS + XO_ + p * XS_ + h * 1024);
    }
    VMW(0); sig(SY(HS1));
    service();
    wait_ge<4>(SY(HS1), 4);
    __builtin_amdgcn_s_setprio(CPRIO);
    TS(1);
    {
      f4v lo, hi;
      const int qw = b & 3;
      const unsigned cc = L_CONV + 2 * CVS;
      if (qw == 0) conv_quarter<0>(cc, lane, h, lo, hi);
      else if (qw == 1) conv_quarter<1>(cc, lane, h, lo, hi);
      else if (qw == 2) conv_quarter<2>(cc, lane, h, lo, hi);
      else conv_quarter<3>(cc, lane, h, lo, hi);
      if (h != 0) lw128(L_CRED + (h - 1) * 1024 + lane * 16, __builtin_bit_cast(u4v, lo));
      if (h != 2) lw128(L_CRED + 3072 + (h == 3 ? 2 : h) * 1024 + lane * 16, __builtin_bit_cast(u4v, hi));
      sig(SY(HS2));
      f4v own = h == 0 ? lo : hi;
      TS(24);
      service();
      const unsigned ca = L_CONV + (h >> 1) * CVS;
      if (h & 1) conv_full<0x3B>(rs, ca, lane, zoff, chA + (h >> 1), service);
      else conv_full<0xC4>(rs, ca, lane, zoff, chA + (h >> 1), service);
      if (!(h & 1)) {
        wait_ge<4>(SY(HS2), 4);
#pragma unroll
        for (int n = 0; n < 3; ++n) {
          const f4v x = __builtin_bit_cast(f4v, lr128(L_CRED + h * 1536 + n * 1024 + lane * 16));
#pragma unroll
          for (int e = 0; e < 4; ++e) own[e] = fadd1(own[e], x[e]);
        }
        conv_store(rs, zoff, h == 0 ? qw : 7 - qw, chC, own);
      }
    }
    __builtin_amdgcn_s_setprio(2);
    sig(SY(HS3));
    TS(8);
    VMW(0);
    if (sig_rtn(SY(PUBY)) == 3 && lane < 32) gst32(ws, WS_FY + (hg * 32 + lane) * 128 + (b & 31) * 4, tag);
    service();
    TS(2);
    TS(27);
    _Pragma("unroll 1") for (unsigned s = 0; s < SPIN_CAP; ++s) { if (rf == 8) break; service(); if (!(rf & 1)) __builtin_amdgcn_s_sleep(16); }
    TS(25);
    wait_words<32>(SY(PROG + 4), 4, 2 * PU, lane);
    if (h == 0) {
      __builtin_amdgcn_s_sleep(FYDELAY);
      poll<FYP>(ws, WS_FY + b * 128 + (lane & 31) * 4, tag, [] {});
      if (lane == 0) lds_st(SY(FYOK), 1u);
    }
    wait_ge<8>(SY(FYOK), 1);
    TS(26);
    {
      const char* src = ws + WS_Z + (unsigned)(t * 576 + 72 * hg) * 128;
      dma_sc1(src + h * 1024, l16, L_Z + h * 1024); dma_sc1(src + 4096 + h * 1024, l16, L_Z + 4096 + h * 1024);
      if (h == 0) dma_sc1(src + 8192, l16, L_Z + 8192);
    }
    VMW(0); sig(SY(HS4));
    wait_ge<4>(SY(HS4), 4);
    {
      const unsigned za = L_Z + (8 * g + ((lane >> 2) & 3)) * 128 + (4 * h + (lane & 3)) * 8;
      const unsigned zb = L_Z + (64 + ((lane >> 2) & 3)) * 128 + (4 * g + (lane & 3)) * 8;
      gzv[0] = ltr2(za, za + 512); gzv[1] = ltr2(za + 4096, za + 4096 + 512); gzv[2] = ltr2(zb, zb + 512);
      sig(SY(HS5));
    }
    TS(10);
    wait_ge<16>(SY(RDYW), 4);
  }

  {
    const int i = lane & 15, g = lane >> 4, q = (lane >> 2) & 3, pp = lane & 3;
    const unsigned col = ng < 2 ? 32 * ng + 4 * pp : 64 + 4 * (pp & 1);
    const unsigned wa = opq(L_WO + (18 * (g >> 1) + 4 * (g & 1) + q) * 144 + col * 2), wb = opq(wa + 41472);
    const unsigned wr = opq(L_WO + (72 * g + 18 * (q >> 1) + 16 + (q & 1)) * 144 + col * 2);
    const unsigned wr1 = g == 3 ? wr + 41472 + 5184 - L_WO - 77760 + L_CRED : wr + 41472 + 5184;
    const unsigned oa = L_O + ((mt >> 1) * 36 + (g >> 1)) * 1024 + (g & 1) * 512 + (16 * (mt & 1) + i) * 16;
    const unsigned op = (unsigned)((64 * t + 16 * mt + i) * 1152 + 576 + 72 * hg + 32 * ng + 4 * g);
    auto wfrag = [&](int s, int nt) -> u4v {
      if (s < 16) { const unsigned a = (s < 8 ? wa + s * 5184 : s < 15 ? wb + (s - 8) * 5184 : wb + (L_CRED - L_WO - 41472)) + 32 * nt; return ltr2(a, a + 1152); }
      return s == 16 ? ltr2(wr + 32 * nt, wr + 5184 + 32 * nt) : ltr2(wr + 41472 + 32 * nt, wr1 + 32 * nt);
    };
    const unsigned pc = (w & 1) * 36864 + (w >> 1) * 2048;
    const char* osrc = ws + WS_O + (unsigned)t * 73728 + pc;
    auto fetch_o = [&](int n) {
      asm volatile("s_mov_b32 m0, %2\n\ts_nop 0\n\tglobal_load_lds_dwordx4 %0, %1 sc1\n\tglobal_load_lds_dwordx4 %0, %1 offset:1024 sc1"
                   :: "v"(lane * 16), "s"(osrc + n * 12288), "s"(L_O + pc + n * 12288) : "memory");
    };
    if (ng < 2) {
      u4v wf[2][18];
      __builtin_amdgcn_s_setprio(0);
#pragma unroll
      for (int s = 0; s < 18; ++s) {
        wf[0][s] = wfrag(s, 0); wf[1][s] = wfrag(s, 1);
        SB();
        if (ng == 0) __builtin_amdgcn_s_sleep(WPACE);
      }
      if (ng == 0) wait_words<8>(SY(PROG + 4), 4, 4 * PU, lane);
      if (w == 4) {
        const unsigned tag = lds_ld(SY(TAGW));
        const int fl = lane & 31;
        const unsigned fo = WS_FO + t * 1024 + hg * 128 + ((fl >> 2) == hg ? (fl ^ 4) : fl) * 4;
        __builtin_amdgcn_s_sleep(ODELAY);
        TS(28);
        poll<4>(ws, fo, tag, [] {});
        wait_ge(SY(HS5), 4); wait_ge(SY(OPUB), 4);
        if (lane == 0) lds_st(SY(OOK), 1u);
        TS(29);
      }
      wait_ge(SY(OOK), 1);
      fetch_o(0); fetch_o(1);
      TS(12);
      VMW(2);
      barrier();
      fetch_o(2);
      TS(13);
      f4v a0 = {0, 0, 0, 0}, a1 = {0, 0, 0, 0};
      u4v of[18];
      of[0] = lr128(oa); of[1] = lr128(oa + 2048);
#pragma unroll
      for (int s = 0; s < 18; ++s) {
        if (s == 4) { VMW(2); asm volatile("s_barrier" ::: "memory"); }
        if (s == 10) { VMW(0); asm volatile("s_barrier" ::: "memory"); }
        if (s + 2 < 18) of[s + 2] = lr128(oa + (s + 2) * 2048);
        SB();
        a0 = mf16(wf[0][s], of[s], a0); a1 = mf16(wf[1][s], of[s], a1);
        SB();
      }
      ost64(ro, op, u2v{pk(a0[0], a0[1]), pk(a0[2], a0[3])});
      ost64(ro, op + 16, u2v{pk(a1[0], a1[1]), pk(a1[2], a1[3])});
      TS(14);
    } else {
      u4v wf[18];
#pragma unroll
      for (int s = 0; s < 18; ++s) wf[s] = wfrag(s, 0);
      u4v go[3];
      wait_words<8>(SY(PROG + 4), 4, 4 * PU, lane);
#pragma unroll
      for (int n = 0; n < 3; ++n)
#pragma unroll
        for (int e = 0; e < 4; ++e) go[n][e] = pk(blo(gqv[n][e]) * blo(gzv[n][e]), bhi(gqv[n][e]) * bhi(gzv[n][e]));
      wait_ge(SY(OOK), 1);
      fetch_o(0); fetch_o(1);
      f4v a0 = {0, 0, 0, 0};
      u4v of[18];
#pragma unroll
      for (int n = 0; n < 3; ++n) {
        if (n < 2) VMW(2); else VMW(0);
        asm volatile("s_barrier" ::: "memory");
        if (n == 0) fetch_o(2);
        if (n == 2) {
          ost128(ro, goo[0], go[0]); ost128(ro, goo[0] + 32, go[1]);
          if (w == 8) ost128(ro, goo[1], go[2]);
        }
#pragma unroll
        for (int s = 6 * n; s < 6 * n + 6; ++s) of[s] = lr128(oa + s * 2048);
        SB();
#pragma unroll
        for (int s = 6 * n; s < 6 * n + 6; ++s) a0 = mf16(wf[s], of[s], a0);
        SB();
      }
      if (g < 2) ost64(ro, op, u2v{pk(a0[0], a0[1]), pk(a0[2], a0[3])});
    }
    TSDUMP();
  }
}

extern "C" void kernel_launch(void* const* d_in, const int* in_sizes, int n_in, void* d_out, int out_size, void* d_ws, size_t ws_size,
                              hipStream_t stream) {
  (void)hipFuncSetAttribute((const void*)fused_h3_mqa, hipFuncAttributeMaxDynamicSharedMemorySize, LDS_BYTES);
  fused_h3_mqa<<<256, 768, LDS_BYTES, stream>>>((const u16*)d_in[0], (const u16*)d_in[1], (const u16*)d_in[2], (const u16*)d_in[3],
      (const u16*)d_in[4], (const u16*)d_in[5], (const u16*)d_in[6], (const u16*)d_in[7], (const u16*)d_in[8], (const u16*)d_in[9],
      (const u16*)d_in[10], (u16*)d_out, (char*)d_ws);
}
```
